```python
import torch

def _softmax(x, axis=-1):
    return torch.softmax(x, dim=axis)
def _gelu(x):
    return torch.nn.functional.gelu(x, approximate="tanh")
def _silu(x):
    return torch.nn.functional.silu(x)
def _sigmoid(x):
    return torch.sigmoid(x)
def _layernorm(x, axis=-1, eps=1e-5):
    m = x.mean(dim=axis, keepdim=True)
    v = x.var(dim=axis, keepdim=True, unbiased=False)
    return (x - m) / torch.sqrt(v + eps)
def _scatter_add(tgt, idx, src, axis=0):
    return tgt.clone().index_add_(axis, idx, src)
def _segment_sum(src, seg, n):
    out = torch.zeros((n,) + src.shape[1:], dtype=src.dtype)
    return out.index_add_(0, seg, src)
def _pad(x, pw):
    flat = tuple(v for pair in reversed(pw) for v in pair)
    return torch.nn.functional.pad(x, flat)

kernel_name = 'gated_mix__softmax_full_mha__dense_activation_squared_relu__prompt'

def setup_inputs():

    _w = (lambda a: (lambda t: t.to(torch.bfloat16) if t.is_floating_point() else t)(torch.as_tensor(a)))
    def _A_setup():
        B, S, D = 1, 2048, 640
        return {
            "x": _w((lambda *s: torch.randn(*s, dtype=torch.bfloat16) * 0.05)(B, S, D)),
            "wqkv": _w((lambda *s: torch.randn(*s, dtype=torch.bfloat16) * 0.8)(D, 3 * D)),
            "wo": _w((lambda *s: torch.randn(*s, dtype=torch.bfloat16) * 0.1)(D, D)),
        }
    _A_in = _A_setup()

    def _B_setup():
        B, S, D, F = 1, 2048, 640, 1536
        return {
            "x": _w((lambda *s: torch.randn(*s, dtype=torch.bfloat16) * 0.05)(B, S, D)),
            "w1": _w((lambda *s: torch.randn(*s, dtype=torch.bfloat16) * 0.1)(D, F)),
            "b1": _w((lambda *s: torch.randn(*s, dtype=torch.bfloat16) * 0.1)(F)),
            "w2": _w((lambda *s: torch.randn(*s, dtype=torch.bfloat16) * 0.1)(F, D)),
            "b2": _w((lambda *s: torch.randn(*s, dtype=torch.bfloat16) * 0.05)(D)),
        }
    _B_in = _B_setup()

    return {
        "A_x": _A_in["x"],
        "A_wqkv": _A_in["wqkv"],
        "A_wo": _A_in["wo"],
        "B_x": _B_in["x"],
        "B_w1": _B_in["w1"],
        "B_b1": _B_in["b1"],
        "B_w2": _B_in["w2"],
        "B_b2": _B_in["b2"],
        "gate_w": _w((lambda *s: torch.randn(*s, dtype=torch.bfloat16) * 0.05)(640) * 16.0),
    }

def reference(A_x, A_wqkv, A_wo, B_x, B_w1, B_b1, B_w2, B_b2, gate_w):
    def _A_ref(x, wqkv, wo):
        B, S, D = x.shape
        H = 32; Dh = D // H
        qkv = (lambda x, *d: x.permute(*d))((x @ wqkv).reshape(B, S, 3, H, Dh), 2, 0, 3, 1, 4)
        q, k, v = qkv[0], qkv[1], qkv[2]
        scale = Dh ** -0.5
        scores = torch.einsum("bhqd,bhkd->bhqk", q, k) * scale
        p = _softmax(scores, axis=-1)
        o = torch.einsum("bhqk,bhkd->bhqd", p, v)
        return (lambda x, *d: x.permute(*d))(o, 0, 2, 1, 3).reshape(B, S, D) @ wo

    def _B_ref(x, w1, b1, w2, b2):
        h = torch.einsum("bsd,df->bsf", x, w1) + b1
        zero = torch.zeros_like(h)
        r = torch.where(h > zero, h, zero)
        h = r * r
        out = torch.einsum("bsf,fd->bsd", h, w2) + b2
        return out

    x = A_x
    g = _sigmoid((x * gate_w).sum(-1, keepdims=True))
    a = _A_ref(x, A_wqkv, A_wo)
    b = _B_ref(x, B_w1, B_b1, B_w2, B_b2)
    return g * a + (1 - g) * b
```

```cpp
#include <hip/hip_runtime.h>
#include <hip/hip_bf16.h>

typedef __attribute__((ext_vector_type(8))) __bf16 bf16x8;
typedef __attribute__((ext_vector_type(2))) __bf16 bf16x2;
typedef __attribute__((ext_vector_type(2))) float f32x2;
typedef __attribute__((ext_vector_type(4))) float f32x4;
typedef __attribute__((ext_vector_type(16))) float f32x16;
typedef __attribute__((ext_vector_type(32))) float f32x32;
typedef __attribute__((ext_vector_type(2))) unsigned u32x2;
typedef __attribute__((ext_vector_type(4))) unsigned u32x4;
typedef __attribute__((ext_vector_type(8))) unsigned u32x8;
typedef __attribute__((ext_vector_type(16))) unsigned u32x16;
#define LDS __attribute__((address_space(3)))
#define DI __device__ __forceinline__

constexpr unsigned KVT = 2688;
constexpr unsigned WS_Q = 0;
constexpr unsigned WS_KV = WS_Q + 16 * 64 * 2560;
constexpr unsigned WS_H = WS_KV + 32 * 64 * KVT;
constexpr unsigned WS_W = WS_H + 64 * 96 * 1024;
constexpr unsigned WS_O = WS_W + 20 * 136 * 1024;
constexpr unsigned WS_G = WS_O + 64 * 80 * 512;
constexpr unsigned WS_F = WS_G + 2048 * 4;
constexpr unsigned F2O = 16384, HBO = 40960;
constexpr unsigned LEAD = 198;

constexpr int LDS_BYTES = 163840;
constexpr unsigned SL = 34816, EB = 4 * SL;
constexpr unsigned SLOT = 21504, XCH = SLOT + 81920;

DI unsigned cvtpk(float lo, float hi) {
  f32x2 v = {lo, hi};
  return __builtin_bit_cast(unsigned, __builtin_convertvector(v, bf16x2));
}
DI f32x16 mfma(bf16x8 a, bf16x8 b, f32x16 c) { return __builtin_amdgcn_mfma_f32_32x32x16_bf16(a, b, c, 0, 0, 0); }
DI unsigned swap23(unsigned t) { return (t & ~12) | ((t & 4) << 1) | ((t & 8) >> 1); }
DI void st16(char* base, unsigned off, u32x4 v) {
  asm volatile("global_store_dwordx4 %0, %1, %2 sc1\n\ts_nop 1" :: "v"(off), "v"(v), "s"(base) : "memory");
}
DI void st8(char* base, unsigned off, u32x2 v) {
  asm volatile("global_store_dwordx2 %0, %1, %2 sc1" :: "v"(off), "v"(v), "s"(base) : "memory");
}
DI unsigned lane_id() {
  unsigned l;
  asm volatile("v_mbcnt_lo_u32_b32 %0, -1, 0\n\tv_mbcnt_hi_u32_b32 %0, -1, %0" : "=v"(l));
  return l;
}
DI float upper_half(float v) {
  return __uint_as_float(__builtin_amdgcn_permlane32_swap(__float_as_uint(v), __float_as_uint(v), false, false)[1]);
}

DI unsigned oword(float a, float b) { return __builtin_elementwise_min(cvtpk(a, b), 0xfffffffeu); }

DI void mark_empty(const char* ws, unsigned base, unsigned l16) {
  unsigned a;
  asm volatile("v_add_u32 %0, %1, %2" : "=v"(a) : "s"(base), "v"(l16));
  u32x4 m = {~0u, ~0u, ~0u, ~0u};
  asm volatile("global_store_dwordx4 %0, %1, %2 sc1\n\ts_mov_b64 exec, 0xffff\n\tglobal_store_dwordx4 %0, %1, %2 offset:1024 sc1\n\t"
               "s_mov_b64 exec, -1\n\ts_nop 1" :: "v"(a), "v"(m), "s"(ws) : "memory");
}

DI f32x16 attn_exact(const char* ws, unsigned qa, unsigned qta, unsigned kv, unsigned t, unsigned hh) {
  u32x4 q;
  u32x2 qt;
  asm volatile("global_load_dwordx4 %0, %2, %4 sc1\n\tglobal_load_dwordx2 %1, %3, %4 sc1\n\ts_waitcnt vmcnt(0)"
               : "=&v"(q), "=&v"(qt) : "v"(qa), "v"(qta), "s"(ws) : "memory");
  const u32x4 qx = {hh ? 0u : qt[0], hh ? 0u : qt[1], 0u, 0u};
  f32x16 o = (f32x16)(0.f);
  float m = -__builtin_inff();
  unsigned ka = kv + hh * 512 + t * 16, kt = kv + 1024 + t * 8;
  unsigned va = kv + 1280 + hh * 8 + (t < 20 ? t * 16 : t == 20 ? 640 : 656), vb = va + (t < 20 ? 320 : 0);
  for (int tile = 0; tile < 64; tile++, ka += KVT, kt += KVT, va += KVT, vb += KVT) {
    u32x4 k;
    u32x2 k2, v[4];
    asm volatile("global_load_dwordx4 %0, %6, %9 sc1\n\tglobal_load_dwordx2 %1, %7, %9 sc1\n\t"
                 "global_load_dwordx2 %2, %8, %9 sc1\n\tglobal_load_dwordx2 %3, %10, %9 sc1\n\t"
                 "global_load_dwordx2 %4, %8, %9 offset:704 sc1\n\tglobal_load_dwordx2 %5, %10, %9 offset:704 sc1\n\ts_waitcnt vmcnt(0)"
                 : "=&v"(k), "=&v"(k2), "=&v"(v[0]), "=&v"(v[1]), "=&v"(v[2]), "=&v"(v[3])
                 : "v"(ka), "v"(kt), "v"(va), "s"(ws), "v"(vb) : "memory");
    const u32x4 kx = {hh ? 0u : k2[0], hh ? 0u : k2[1], 0u, 0u};
    f32x16 s = mfma(__builtin_bit_cast(bf16x8, k), __builtin_bit_cast(bf16x8, q), (f32x16)(0.f));
    s = mfma(__builtin_bit_cast(bf16x8, kx), __builtin_bit_cast(bf16x8, qx), s);
    float mx = m;
#pragma unroll
    for (int i = 0; i < 16; i++) mx = fmaxf(mx, s[i]);
    const auto sw = __builtin_amdgcn_permlane32_swap(__float_as_uint(mx), __float_as_uint(mx), false, false);
    mx = fmaxf(__uint_as_float(sw[0]), __uint_as_float(sw[1]));
    o *= __builtin_amdgcn_exp2f(m - mx);
    m = mx;
#pragma unroll
    for (int sh = 0; sh < 2; sh++) {
      const u32x4 a = {v[2 * sh][0], v[2 * sh][1], v[2 * sh + 1][0], v[2 * sh + 1][1]};
      u32x4 p;
#pragma unroll
      for (int i = 0; i < 4; i++)
        p[i] = cvtpk(__builtin_amdgcn_exp2f(s[8 * sh + 2 * i] - m), __builtin_amdgcn_exp2f(s[8 * sh + 2 * i + 1] - m));
      o = mfma(__builtin_bit_cast(bf16x8, a), __builtin_bit_cast(bf16x8, p), o);
    }
  }
  return o;
}

#define ST_CL
#define CYC0()
#define STAMP(i)

#define A0 \
  "s_nop 4\n\t" \
  "s_bitcmp1_b32 %[fl], 4\n\t" \
  "s_cbranch_scc1 Sa00_%=\n\t" \
  "s_add_u32 m0, %[w2k], 69632\n\t" \
  "s_nop 0\n\t" \
  "global_load_lds_dwordx4 v64, s[40:41]\n\t" \
  "global_load_lds_dwordx4 v65, s[40:41] offset:1024\n\t" \
  "s_add_u32 s40, s40, 128\n\t" \
  "s_addc_u32 s41, s41, 0\n\t" \
  "s_add_u32 m0, %[w2k], 86016\n\t" \
  "s_nop 0\n\t" \
  "global_load_lds_dwordx4 v66, s[42:43]\n\t" \
  "global_load_lds_dwordx4 v67, s[42:43] offset:1024\n\t" \
  "s_add_u32 s42, s42, 245760\n\t" \
  "s_addc_u32 s43, s43, 0\n\t" \
  "s_add_u32 m0, %[w2k], 104448\n\t" \
  "s_nop 0\n\t" \
  "global_load_lds_dwordx4 v64, s[40:41]\n\t" \
  "global_load_lds_dwordx4 v65, s[40:41] offset:1024\n\t" \
  "s_add_u32 s40, s40, 128\n\t" \
  "s_addc_u32 s41, s41, 0\n\t" \
  "s_add_u32 m0, %[w2k], 120832\n\t" \
  "s_nop 0\n\t" \
  "global_load_lds_dwordx4 v66, s[42:43]\n\t" \
  "global_load_lds_dwordx4 v67, s[42:43] offset:1024\n\t" \
  "s_add_u32 s42, s42, 245760\n\t" \
  "s_addc_u32 s43, s43, 0\n\t" \
  "s_add_u32 m0, %[w2k], 0\n\t" \
  "s_nop 0\n\t" \
  "global_load_lds_dwordx4 v64, s[40:41]\n\t" \
  "global_load_lds_dwordx4 v65, s[40:41] offset:1024\n\t" \
  "s_add_u32 s40, s40, 128\n\t" \
  "s_addc_u32 s41, s41, 0\n\t" \
  "s_add_u32 m0, %[w2k], 16384\n\t" \
  "s_nop 0\n\t" \
  "global_load_lds_dwordx4 v66, s[42:43]\n\t" \
  "global_load_lds_dwordx4 v67, s[42:43] offset:1024\n\t" \
  "s_add_u32 s42, s42, 245760\n\t" \
  "s_addc_u32 s43, s43, 0\n\t" \
  "s_branch Ea0_%=\n\t" \
  "Sa00_%=:\n\t" \
  "s_bitcmp1_b32 %[fl], 5\n\t" \
  "s_cbranch_scc0 La021_%=\n\t" \
  "v_lshrrev_b32 v4, 1, %[w2k]\n\t" \
  "v_mov_b32 v0, 0\n\t" \
  "v_mov_b32 v1, 0\n\t" \
  "v_mov_b32 v2, 0\n\t" \
  "v_mov_b32 v3, 0\n\t" \
  "v_mad_u32_u24 v4, v4, 5, v92\n\t" \
  "v_add_u32 v4, 2048, v4\n\t" \
  "global_store_dwordx4 v4, v[0:3], %[fb] offset:-2048 sc1\n\t" \
  "global_store_dwordx4 v4, v[0:3], %[fb] offset:-1024 sc1\n\t" \
  "global_store_dwordx4 v4, v[0:3], %[fb] offset:0 sc1\n\t" \
  "global_store_dwordx4 v4, v[0:3], %[fb] offset:1024 sc1\n\t" \
  "global_store_dwordx4 v4, v[0:3], %[fb] offset:2048 sc1\n\t" \
  "La021_%=:\n\t" \
  "s_mov_b32 m0, 32768\n\t" \
  "s_nop 0\n\t" \
  "global_load_lds_dwordx4 v92, %[gw]\n\t" \
  "global_load_lds_dwordx4 v91, %[gw] offset:1024\n\t" \
  "s_add_u32 m0, %[w2k], 69632\n\t" \
  "s_nop 0\n\t" \
  "global_load_lds_dwordx4 v64, s[40:41]\n\t" \
  "global_load_lds_dwordx4 v65, s[40:41] offset:1024\n\t" \
  "s_add_u32 s40, s40, 128\n\t" \
  "s_addc_u32 s41, s41, 0\n\t" \
  "s_add_u32 m0, %[w2k], 104448\n\t" \
  "s_nop 0\n\t" \
  "global_load_lds_dwordx4 v64, s[40:41]\n\t" \
  "global_load_lds_dwordx4 v65, s[40:41] offset:1024\n\t" \
  "s_add_u32 s40, s40, 128\n\t" \
  "s_addc_u32 s41, s41, 0\n\t" \
  "s_add_u32 m0, %[w2k], 0\n\t" \
  "s_nop 0\n\t" \
  "global_load_lds_dwordx4 v64, s[40:41]\n\t" \
  "global_load_lds_dwordx4 v65, s[40:41] offset:1024\n\t" \
  "s_add_u32 s40, s40, 128\n\t" \
  "s_addc_u32 s41, s41, 0\n\t" \
  "Ea0_%=:\n\t"
#define A1a \
  "s_nop 4\n\t" \
  "s_bitcmp1_b32 %[fl], 4\n\t" \
  "s_cbranch_scc1 Sa100_%=\n\t" \
  "s_bitcmp1_b32 %[fl], 3\n\t" \
  "s_cbranch_scc1 Sa101_%=\n\t" \
  "s_bitcmp1_b32 %[fl], 0\n\t" \
  "s_cbranch_scc0 La101_%=\n\t" \
  "global_load_dword v88, v87, %[fb] sc1\n\t" \
  "La101_%=:\n\t" \
  "v_mov_b32 v89, 0\n\t" \
  "s_waitcnt vmcnt(8)\n\t" \
  "s_barrier\n\t" \
  "s_bitcmp1_b32 %[fl], 1\n\t" \
  "s_cbranch_scc0 La102_%=\n\t" \
  "v_add_u32 v89, 1, v89\n\t" \
  "global_store_dword v87, v89, %[fb] sc1\n\t" \
  "La102_%=:\n\t" \
  "ds_read_b128 v[96:99], v68 offset:0\n\t" \
  "ds_read_b64_tr_b16 v[32:33], v72 offset:0\n\t" \
  "ds_read_b64_tr_b16 v[34:35], v72 offset:1024\n\t" \
  "ds_read_b64_tr_b16 v[36:37], v73 offset:0\n\t" \
  "ds_read_b64_tr_b16 v[38:39], v73 offset:1024\n\t" \
  "s_add_u32 m0, %[w2k], 34816\n\t" \
  "s_nop 0\n\t" \
  "global_load_lds_dwordx4 v64, s[40:41]\n\t" \
  "ds_read_b128 v[100:103], v69 offset:0\n\t" \
  "ds_read_b64_tr_b16 v[40:41], v72 offset:4096\n\t" \
  "ds_read_b64_tr_b16 v[42:43], v72 offset:5120\n\t" \
  "ds_read_b64_tr_b16 v[44:45], v73 offset:4096\n\t" \
  "ds_read_b64_tr_b16 v[46:47], v73 offset:5120\n\t" \
  "s_waitcnt lgkmcnt(5)\n\t" \
  "v_mfma_f32_32x32x16_bf16 v[0:15], v[32:35], v[96:99], 0\n\t" \
  "v_mfma_f32_32x32x16_bf16 v[16:31], v[36:39], v[96:99], 0\n\t" \
  "global_load_lds_dwordx4 v65, s[40:41] offset:1024\n\t" \
  "s_add_u32 s40, s40, 128\n\t" \
  "s_addc_u32 s41, s41, 0\n\t" \
  "ds_read_b128 v[104:107], v70 offset:0\n\t" \
  "ds_read_b64_tr_b16 v[32:33], v72 offset:8192\n\t" \
  "ds_read_b64_tr_b16 v[34:35], v72 offset:9216\n\t" \
  "ds_read_b64_tr_b16 v[36:37], v73 offset:8192\n\t" \
  "ds_read_b64_tr_b16 v[38:39], v73 offset:9216\n\t" \
  "s_waitcnt lgkmcnt(5)\n\t" \
  "v_mfma_f32_32x32x16_bf16 v[0:15], v[40:43], v[100:103], v[0:15]\n\t" \
  "v_mfma_f32_32x32x16_bf16 v[16:31], v[44:47], v[100:103], v[16:31]\n\t" \
  "s_add_u32 m0, %[w2k], 51200\n\t" \
  "s_nop 0\n\t" \
  "global_load_lds_dwordx4 v66, s[42:43]\n\t" \
  "ds_read_b128 v[108:111], v71 offset:0\n\t" \
  "ds_read_b64_tr_b16 v[40:41], v72 offset:12288\n\t" \
  "ds_read_b64_tr_b16 v[42:43], v72 offset:13312\n\t" \
  "ds_read_b64_tr_b16 v[44:45], v73 offset:12288\n\t" \
  "ds_read_b64_tr_b16 v[46:47], v73 offset:13312\n\t" \
  "s_waitcnt lgkmcnt(5)\n\t" \
  "v_mfma_f32_32x32x16_bf16 v[0:15], v[32:35], v[104:107], v[0:15]\n\t" \
  "v_mfma_f32_32x32x16_bf16 v[16:31], v[36:39], v[104:107], v[16:31]\n\t" \
  "global_load_lds_dwordx4 v67, s[42:43] offset:1024\n\t" \
  "s_add_u32 s42, s42, 245760\n\t" \
  "s_addc_u32 s43, s43, 0\n\t" \
  "s_waitcnt lgkmcnt(0)\n\t" \
  "s_waitcnt vmcnt(8)\n\t" \
  "s_barrier\n\t" \
  "ds_read_b128 v[112:115], v68 offset:34816\n\t" \
  "ds_read_b64_tr_b16 v[32:33], v72 offset:34816\n\t" \
  "ds_read_b64_tr_b16 v[34:35], v72 offset:35840\n\t" \
  "ds_read_b64_tr_b16 v[36:37], v73 offset:34816\n\t" \
  "ds_read_b64_tr_b16 v[38:39], v73 offset:35840\n\t" \
  "v_mfma_f32_32x32x16_bf16 v[0:15], v[40:43], v[108:111], v[0:15]\n\t" \
  "v_mfma_f32_32x32x16_bf16 v[16:31], v[44:47], v[108:111], v[16:31]\n\t" \
  "s_bitcmp1_b32 %[fl], 1\n\t" \
  "s_cbranch_scc0 La103_%=\n\t" \
  "v_add_u32 v89, 1, v89\n\t" \
  "global_store_dword v87, v89, %[fb] sc1\n\t" \
  "La103_%=:\n\t" \
  "s_add_u32 m0, %[w2k], 69632\n\t" \
  "s_nop 0\n\t" \
  "global_load_lds_dwordx4 v64, s[40:41]\n\t" \
  "ds_read_b128 v[116:119], v69 offset:34816\n\t" \
  "ds_read_b64_tr_b16 v[40:41], v72 offset:38912\n\t" \
  "ds_read_b64_tr_b16 v[42:43], v72 offset:39936\n\t" \
  "ds_read_b64_tr_b16 v[44:45], v73 offset:38912\n\t" \
  "ds_read_b64_tr_b16 v[46:47], v73 offset:39936\n\t" \
  "s_waitcnt lgkmcnt(5)\n\t" \
  "v_mfma_f32_32x32x16_bf16 v[0:15], v[32:35], v[112:115], v[0:15]\n\t" \
  "v_mfma_f32_32x32x16_bf16 v[16:31], v[36:39], v[112:115], v[16:31]\n\t" \
  "global_load_lds_dwordx4 v65, s[40:41] offset:1024\n\t" \
  "s_add_u32 s40, s40, 128\n\t" \
  "s_addc_u32 s41, s41, 0\n\t" \
  "ds_read_b128 v[120:123], v70 offset:34816\n\t" \
  "ds_read_b64_tr_b16 v[32:33], v72 offset:43008\n\t" \
  "ds_read_b64_tr_b16 v[34:35], v72 offset:44032\n\t" \
  "ds_read_b64_tr_b16 v[36:37], v73 offset:43008\n\t" \
  "ds_read_b64_tr_b16 v[38:39], v73 offset:44032\n\t" \
  "s_branch Ea10_%=\n\t" \
  "Sa100_%=:\n\t" \
  "s_bitcmp1_b32 %[fl], 0\n\t" \
  "s_cbranch_scc0 La121_%=\n\t" \
  "global_load_dword v88, v87, %[fb] sc1\n\t" \
  "La121_%=:\n\t" \
  "v_mov_b32 v89, 0\n\t" \
  "s_waitcnt vmcnt(4)\n\t" \
  "s_barrier\n\t" \
  "s_bitcmp1_b32 %[fl], 1\n\t" \
  "s_cbranch_scc0 La122_%=\n\t" \
  "v_add_u32 v89, 1, v89\n\t" \
  "global_store_dword v87, v89, %[fb] sc1\n\t" \
  "La122_%=:\n\t" \
  "ds_read_b128 v[96:99], v68 offset:0\n\t" \
  "ds_read_b128 v[32:35], v72 offset:0\n\t" \
  "s_add_u32 m0, %[w2k], 34816\n\t" \
  "s_nop 0\n\t" \
  "global_load_lds_dwordx4 v64, s[40:41]\n\t" \
  "ds_read_b128 v[100:103], v69 offset:0\n\t" \
  "ds_read_b128 v[40:43], v72 offset:32\n\t" \
  "s_waitcnt lgkmcnt(2)\n\t" \
  "v_mfma_f32_32x32x16_bf16 v[0:15], v[32:35], v[96:99], 0\n\t" \
  "global_load_lds_dwordx4 v65, s[40:41] offset:1024\n\t" \
  "s_add_u32 s40, s40, 128\n\t" \
  "s_addc_u32 s41, s41, 0\n\t" \
  "ds_read_b128 v[104:107], v70 offset:0\n\t" \
  "ds_read_b128 v[32:35], v72 offset:64\n\t" \
  "s_waitcnt lgkmcnt(2)\n\t" \
  "v_mfma_f32_32x32x16_bf16 v[0:15], v[40:43], v[100:103], v[0:15]\n\t" \
  "ds_read_b128 v[108:111], v71 offset:0\n\t" \
  "ds_read_b128 v[40:43], v72 offset:96\n\t" \
  "s_waitcnt lgkmcnt(2)\n\t" \
  "v_mfma_f32_32x32x16_bf16 v[0:15], v[32:35], v[104:107], v[0:15]\n\t" \
  "s_waitcnt lgkmcnt(0)\n\t" \
  "s_waitcnt vmcnt(4)\n\t" \
  "s_barrier\n\t" \
  "ds_read_b128 v[112:115], v68 offset:34816\n\t" \
  "ds_read_b128 v[32:35], v72 offset:128\n\t" \
  "v_mfma_f32_32x32x16_bf16 v[0:15], v[40:43], v[108:111], v[0:15]\n\t" \
  "s_bitcmp1_b32 %[fl], 1\n\t" \
  "s_cbranch_scc0 La123_%=\n\t" \
  "v_add_u32 v89, 1, v89\n\t" \
  "global_store_dword v87, v89, %[fb] sc1\n\t" \
  "La123_%=:\n\t" \
  "s_add_u32 m0, %[w2k], 69632\n\t" \
  "s_nop 0\n\t" \
  "global_load_lds_dwordx4 v64, s[40:41]\n\t" \
  "ds_read_b128 v[116:119], v69 offset:34816\n\t" \
  "ds_read_b128 v[40:43], v72 offset:160\n\t" \
  "s_waitcnt lgkmcnt(2)\n\t" \
  "v_mfma_f32_32x32x16_bf16 v[0:15], v[32:35], v[112:115], v[0:15]\n\t" \
  "global_load_lds_dwordx4 v65, s[40:41] offset:1024\n\t" \
  "s_add_u32 s40, s40, 128\n\t" \
  "s_addc_u32 s41, s41, 0\n\t" \
  "ds_read_b128 v[120:123], v70 offset:34816\n\t" \
  "ds_read_b128 v[32:35], v72 offset:192\n\t" \
  "s_branch Ea10_%=\n\t" \
  "Sa101_%=:\n\t" \
  "s_bitcmp1_b32 %[fl], 0\n\t" \
  "s_cbranch_scc0 La111_%=\n\t" \
  "global_load_dword v88, v87, %[fb] sc1\n\t" \
  "La111_%=:\n\t" \
  "v_mov_b32 v89, 0\n\t" \
  "s_waitcnt vmcnt(8)\n\t" \
  "s_barrier\n\t" \
  "s_bitcmp1_b32 %[fl], 1\n\t" \
  "s_cbranch_scc0 La112_%=\n\t" \
  "v_add_u32 v89, 1, v89\n\t" \
  "global_store_dword v87, v89, %[fb] sc1\n\t" \
  "La112_%=:\n\t" \
  "ds_read_b128 v[96:99], v68 offset:0\n\t" \
  "ds_read_b64_tr_b16 v[32:33], v72 offset:0\n\t" \
  "ds_read_b64_tr_b16 v[34:35], v72 offset:1024\n\t" \
  "ds_read_b64_tr_b16 v[36:37], v73 offset:0\n\t" \
  "ds_read_b64_tr_b16 v[38:39], v73 offset:1024\n\t" \
  "s_add_u32 m0, %[w2k], 34816\n\t" \
  "s_nop 0\n\t" \
  "global_load_lds_dwordx4 v64, s[40:41]\n\t" \
  "ds_read_b128 v[100:103], v69 offset:0\n\t" \
  "ds_read_b64_tr_b16 v[40:41], v72 offset:4096\n\t" \
  "ds_read_b64_tr_b16 v[42:43], v72 offset:5120\n\t" \
  "ds_read_b64_tr_b16 v[44:45], v73 offset:4096\n\t" \
  "ds_read_b64_tr_b16 v[46:47], v73 offset:5120\n\t" \
  "s_waitcnt lgkmcnt(5)\n\t" \
  "v_mfma_f32_32x32x16_bf16 v[0:15], v[96:99], v[32:35], 0\n\t" \
  "v_mfma_f32_32x32x16_bf16 v[16:31], v[96:99], v[36:39], 0\n\t" \
  "global_load_lds_dwordx4 v65, s[40:41] offset:1024\n\t" \
  "s_add_u32 s40, s40, 128\n\t" \
  "s_addc_u32 s41, s41, 0\n\t" \
  "ds_read_b128 v[104:107], v70 offset:0\n\t" \
  "ds_read_b64_tr_b16 v[32:33], v72 offset:8192\n\t" \
  "ds_read_b64_tr_b16 v[34:35], v72 offset:9216\n\t" \
  "ds_read_b64_tr_b16 v[36:37], v73 offset:8192\n\t" \
  "ds_read_b64_tr_b16 v[38:39], v73 offset:9216\n\t" \
  "s_waitcnt lgkmcnt(5)\n\t" \
  "v_mfma_f32_32x32x16_bf16 v[0:15], v[100:103], v[40:43], v[0:15]\n\t" \
  "v_mfma_f32_32x32x16_bf16 v[16:31], v[100:103], v[44:47], v[16:31]\n\t" \
  "s_add_u32 m0, %[w2k], 51200\n\t" \
  "s_nop 0\n\t" \
  "global_load_lds_dwordx4 v66, s[42:43]\n\t" \
  "ds_read_b128 v[108:111], v71 offset:0\n\t" \
  "ds_read_b64_tr_b16 v[40:41], v72 offset:12288\n\t" \
  "ds_read_b64_tr_b16 v[42:43], v72 offset:13312\n\t" \
  "ds_read_b64_tr_b16 v[44:45], v73 offset:12288\n\t" \
  "ds_read_b64_tr_b16 v[46:47], v73 offset:13312\n\t" \
  "s_waitcnt lgkmcnt(5)\n\t" \
  "v_mfma_f32_32x32x16_bf16 v[0:15], v[104:107], v[32:35], v[0:15]\n\t" \
  "v_mfma_f32_32x32x16_bf16 v[16:31], v[104:107], v[36:39], v[16:31]\n\t" \
  "global_load_lds_dwordx4 v67, s[42:43] offset:1024\n\t" \
  "s_add_u32 s42, s42, 245760\n\t" \
  "s_addc_u32 s43, s43, 0\n\t" \
  "s_waitcnt lgkmcnt(0)\n\t" \
  "s_waitcnt vmcnt(8)\n\t" \
  "s_barrier\n\t" \
  "ds_read_b128 v[112:115], v68 offset:34816\n\t" \
  "ds_read_b64_tr_b16 v[32:33], v72 offset:34816\n\t" \
  "ds_read_b64_tr_b16 v[34:35], v72 offset:35840\n\t" \
  "ds_read_b64_tr_b16 v[36:37], v73 offset:34816\n\t" \
  "ds_read_b64_tr_b16 v[38:39], v73 offset:35840\n\t" \
  "v_mfma_f32_32x32x16_bf16 v[0:15], v[108:111], v[40:43], v[0:15]\n\t" \
  "v_mfma_f32_32x32x16_bf16 v[16:31], v[108:111], v[44:47], v[16:31]\n\t" \
  "s_bitcmp1_b32 %[fl], 1\n\t" \
  "s_cbranch_scc0 La113_%=\n\t" \
  "v_add_u32 v89, 1, v89\n\t" \
  "global_store_dword v87, v89, %[fb] sc1\n\t" \
  "La113_%=:\n\t" \
  "s_add_u32 m0, %[w2k], 69632\n\t" \
  "s_nop 0\n\t" \
  "global_load_lds_dwordx4 v64, s[40:41]\n\t" \
  "ds_read_b128 v[116:119], v69 offset:34816\n\t" \
  "ds_read_b64_tr_b16 v[40:41], v72 offset:38912\n\t" \
  "ds_read_b64_tr_b16 v[42:43], v72 offset:39936\n\t" \
  "ds_read_b64_tr_b16 v[44:45], v73 offset:38912\n\t" \
  "ds_read_b64_tr_b16 v[46:47], v73 offset:39936\n\t" \
  "s_waitcnt lgkmcnt(5)\n\t" \
  "v_mfma_f32_32x32x16_bf16 v[0:15], v[112:115], v[32:35], v[0:15]\n\t" \
  "v_mfma_f32_32x32x16_bf16 v[16:31], v[112:115], v[36:39], v[16:31]\n\t" \
  "global_load_lds_dwordx4 v65, s[40:41] offset:1024\n\t" \
  "s_add_u32 s40, s40, 128\n\t" \
  "s_addc_u32 s41, s41, 0\n\t" \
  "ds_read_b128 v[120:123], v70 offset:34816\n\t" \
  "ds_read_b64_tr_b16 v[32:33], v72 offset:43008\n\t" \
  "ds_read_b64_tr_b16 v[34:35], v72 offset:44032\n\t" \
  "ds_read_b64_tr_b16 v[36:37], v73 offset:43008\n\t" \
  "ds_read_b64_tr_b16 v[38:39], v73 offset:44032\n\t" \
  "Ea10_%=:\n\t"
#define A1b \
  "s_nop 4\n\t" \
  "s_bitcmp1_b32 %[fl], 4\n\t" \
  "s_cbranch_scc1 Sa110_%=\n\t" \
  "s_bitcmp1_b32 %[fl], 3\n\t" \
  "s_cbranch_scc1 Sa111_%=\n\t" \
  "s_waitcnt lgkmcnt(5)\n\t" \
  "v_mfma_f32_32x32x16_bf16 v[0:15], v[40:43], v[116:119], v[0:15]\n\t" \
  "v_mfma_f32_32x32x16_bf16 v[16:31], v[44:47], v[116:119], v[16:31]\n\t" \
  "s_add_u32 m0, %[w2k], 86016\n\t" \
  "s_nop 0\n\t" \
  "global_load_lds_dwordx4 v66, s[42:43]\n\t" \
  "ds_read_b128 v[124:127], v71 offset:34816\n\t" \
  "ds_read_b64_tr_b16 v[40:41], v72 offset:47104\n\t" \
  "ds_read_b64_tr_b16 v[42:43], v72 offset:48128\n\t" \
  "ds_read_b64_tr_b16 v[44:45], v73 offset:47104\n\t" \
  "ds_read_b64_tr_b16 v[46:47], v73 offset:48128\n\t" \
  "s_waitcnt lgkmcnt(5)\n\t" \
  "v_mfma_f32_32x32x16_bf16 v[0:15], v[32:35], v[120:123], v[0:15]\n\t" \
  "v_mfma_f32_32x32x16_bf16 v[16:31], v[36:39], v[120:123], v[16:31]\n\t" \
  "global_load_lds_dwordx4 v67, s[42:43] offset:1024\n\t" \
  "s_add_u32 s42, s42, 245760\n\t" \
  "s_addc_u32 s43, s43, 0\n\t" \
  "s_waitcnt lgkmcnt(0)\n\t" \
  "s_waitcnt vmcnt(8)\n\t" \
  "s_barrier\n\t" \
  "v_add_u32 v68, -69632, v68\n\t" \
  "v_add_u32 v69, -69632, v69\n\t" \
  "v_add_u32 v70, -69632, v70\n\t" \
  "v_add_u32 v71, -69632, v71\n\t" \
  "v_add_u32 v72, -69632, v72\n\t" \
  "v_add_u32 v73, -69632, v73\n\t" \
  "ds_read_b128 v[128:131], v68 offset:0\n\t" \
  "ds_read_b64_tr_b16 v[32:33], v72 offset:0\n\t" \
  "ds_read_b64_tr_b16 v[34:35], v72 offset:1024\n\t" \
  "ds_read_b64_tr_b16 v[36:37], v73 offset:0\n\t" \
  "ds_read_b64_tr_b16 v[38:39], v73 offset:1024\n\t" \
  "v_mfma_f32_32x32x16_bf16 v[0:15], v[40:43], v[124:127], v[0:15]\n\t" \
  "v_mfma_f32_32x32x16_bf16 v[16:31], v[44:47], v[124:127], v[16:31]\n\t" \
  "s_bitcmp1_b32 %[fl], 1\n\t" \
  "s_cbranch_scc0 La104_%=\n\t" \
  "v_add_u32 v89, 1, v89\n\t" \
  "global_store_dword v87, v89, %[fb] sc1\n\t" \
  "La104_%=:\n\t" \
  "s_add_u32 m0, %[w2k], 104448\n\t" \
  "s_nop 0\n\t" \
  "global_load_lds_dwordx4 v64, s[40:41]\n\t" \
  "ds_read_b128 v[132:135], v69 offset:0\n\t" \
  "ds_read_b64_tr_b16 v[40:41], v72 offset:4096\n\t" \
  "ds_read_b64_tr_b16 v[42:43], v72 offset:5120\n\t" \
  "ds_read_b64_tr_b16 v[44:45], v73 offset:4096\n\t" \
  "ds_read_b64_tr_b16 v[46:47], v73 offset:5120\n\t" \
  "s_branch Ea11_%=\n\t" \
  "Sa110_%=:\n\t" \
  "s_waitcnt lgkmcnt(2)\n\t" \
  "v_mfma_f32_32x32x16_bf16 v[0:15], v[40:43], v[116:119], v[0:15]\n\t" \
  "ds_read_b128 v[124:127], v71 offset:34816\n\t" \
  "ds_read_b128 v[40:43], v72 offset:224\n\t" \
  "s_waitcnt lgkmcnt(2)\n\t" \
  "v_mfma_f32_32x32x16_bf16 v[0:15], v[32:35], v[120:123], v[0:15]\n\t" \
  "s_waitcnt lgkmcnt(0)\n\t" \
  "s_waitcnt vmcnt(4)\n\t" \
  "s_barrier\n\t" \
  "v_add_u32 v68, -69632, v68\n\t" \
  "v_add_u32 v69, -69632, v69\n\t" \
  "v_add_u32 v70, -69632, v70\n\t" \
  "v_add_u32 v71, -69632, v71\n\t" \
  "ds_read_b128 v[128:131], v68 offset:0\n\t" \
  "ds_read_b128 v[32:35], v72 offset:256\n\t" \
  "v_mfma_f32_32x32x16_bf16 v[0:15], v[40:43], v[124:127], v[0:15]\n\t" \
  "s_bitcmp1_b32 %[fl], 1\n\t" \
  "s_cbranch_scc0 La124_%=\n\t" \
  "v_add_u32 v89, 1, v89\n\t" \
  "global_store_dword v87, v89, %[fb] sc1\n\t" \
  "La124_%=:\n\t" \
  "s_add_u32 m0, %[w2k], 104448\n\t" \
  "s_nop 0\n\t" \
  "global_load_lds_dwordx4 v64, s[40:41]\n\t" \
  "ds_read_b128 v[132:135], v69 offset:0\n\t" \
  "ds_read_b128 v[40:43], v72 offset:288\n\t" \
  "s_branch Ea11_%=\n\t" \
  "Sa111_%=:\n\t" \
  "s_waitcnt lgkmcnt(5)\n\t" \
  "v_mfma_f32_32x32x16_bf16 v[0:15], v[116:119], v[40:43], v[0:15]\n\t" \
  "v_mfma_f32_32x32x16_bf16 v[16:31], v[116:119], v[44:47], v[16:31]\n\t" \
  "s_add_u32 m0, %[w2k], 86016\n\t" \
  "s_nop 0\n\t" \
  "global_load_lds_dwordx4 v66, s[42:43]\n\t" \
  "ds_read_b128 v[124:127], v71 offset:34816\n\t" \
  "ds_read_b64_tr_b16 v[40:41], v72 offset:47104\n\t" \
  "ds_read_b64_tr_b16 v[42:43], v72 offset:48128\n\t" \
  "ds_read_b64_tr_b16 v[44:45], v73 offset:47104\n\t" \
  "ds_read_b64_tr_b16 v[46:47], v73 offset:48128\n\t" \
  "s_waitcnt lgkmcnt(5)\n\t" \
  "v_mfma_f32_32x32x16_bf16 v[0:15], v[120:123], v[32:35], v[0:15]\n\t" \
  "v_mfma_f32_32x32x16_bf16 v[16:31], v[120:123], v[36:39], v[16:31]\n\t" \
  "global_load_lds_dwordx4 v67, s[42:43] offset:1024\n\t" \
  "s_add_u32 s42, s42, 245760\n\t" \
  "s_addc_u32 s43, s43, 0\n\t" \
  "s_waitcnt lgkmcnt(0)\n\t" \
  "s_waitcnt vmcnt(8)\n\t" \
  "s_barrier\n\t" \
  "v_add_u32 v68, -69632, v68\n\t" \
  "v_add_u32 v69, -69632, v69\n\t" \
  "v_add_u32 v70, -69632, v70\n\t" \
  "v_add_u32 v71, -69632, v71\n\t" \
  "v_add_u32 v72, -69632, v72\n\t" \
  "v_add_u32 v73, -69632, v73\n\t" \
  "ds_read_b128 v[128:131], v68 offset:0\n\t" \
  "ds_read_b64_tr_b16 v[32:33], v72 offset:0\n\t" \
  "ds_read_b64_tr_b16 v[34:35], v72 offset:1024\n\t" \
  "ds_read_b64_tr_b16 v[36:37], v73 offset:0\n\t" \
  "ds_read_b64_tr_b16 v[38:39], v73 offset:1024\n\t" \
  "v_mfma_f32_32x32x16_bf16 v[0:15], v[124:127], v[40:43], v[0:15]\n\t" \
  "v_mfma_f32_32x32x16_bf16 v[16:31], v[124:127], v[44:47], v[16:31]\n\t" \
  "s_bitcmp1_b32 %[fl], 1\n\t" \
  "s_cbranch_scc0 La114_%=\n\t" \
  "v_add_u32 v89, 1, v89\n\t" \
  "global_store_dword v87, v89, %[fb] sc1\n\t" \
  "La114_%=:\n\t" \
  "s_add_u32 m0, %[w2k], 104448\n\t" \
  "s_nop 0\n\t" \
  "global_load_lds_dwordx4 v64, s[40:41]\n\t" \
  "ds_read_b128 v[132:135], v69 offset:0\n\t" \
  "ds_read_b64_tr_b16 v[40:41], v72 offset:4096\n\t" \
  "ds_read_b64_tr_b16 v[42:43], v72 offset:5120\n\t" \
  "ds_read_b64_tr_b16 v[44:45], v73 offset:4096\n\t" \
  "ds_read_b64_tr_b16 v[46:47], v73 offset:5120\n\t" \
  "Ea11_%=:\n\t"
#define A1c \
  "s_nop 4\n\t" \
  "s_bitcmp1_b32 %[fl], 4\n\t" \
  "s_cbranch_scc1 Sa120_%=\n\t" \
  "s_bitcmp1_b32 %[fl], 3\n\t" \
  "s_cbranch_scc1 Sa121_%=\n\t" \
  "s_waitcnt lgkmcnt(5)\n\t" \
  "v_mfma_f32_32x32x16_bf16 v[0:15], v[32:35], v[128:131], v[0:15]\n\t" \
  "v_mfma_f32_32x32x16_bf16 v[16:31], v[36:39], v[128:131], v[16:31]\n\t" \
  "s_add_u32 m0, %[w2k], 104448\n\t" \
  "s_nop 0\n\t" \
  "global_load_lds_dwordx4 v65, s[40:41] offset:1024\n\t" \
  "s_add_u32 s40, s40, 128\n\t" \
  "s_addc_u32 s41, s41, 0\n\t" \
  "ds_read_b128 v[136:139], v70 offset:0\n\t" \
  "ds_read_b64_tr_b16 v[32:33], v72 offset:8192\n\t" \
  "ds_read_b64_tr_b16 v[34:35], v72 offset:9216\n\t" \
  "ds_read_b64_tr_b16 v[36:37], v73 offset:8192\n\t" \
  "ds_read_b64_tr_b16 v[38:39], v73 offset:9216\n\t" \
  "s_waitcnt lgkmcnt(5)\n\t" \
  "v_mfma_f32_32x32x16_bf16 v[0:15], v[40:43], v[132:135], v[0:15]\n\t" \
  "v_mfma_f32_32x32x16_bf16 v[16:31], v[44:47], v[132:135], v[16:31]\n\t" \
  "s_add_u32 m0, %[w2k], 120832\n\t" \
  "s_nop 0\n\t" \
  "global_load_lds_dwordx4 v66, s[42:43]\n\t" \
  "ds_read_b128 v[140:143], v71 offset:0\n\t" \
  "ds_read_b64_tr_b16 v[40:41], v72 offset:12288\n\t" \
  "ds_read_b64_tr_b16 v[42:43], v72 offset:13312\n\t" \
  "ds_read_b64_tr_b16 v[44:45], v73 offset:12288\n\t" \
  "ds_read_b64_tr_b16 v[46:47], v73 offset:13312\n\t" \
  "s_branch Ea12_%=\n\t" \
  "Sa120_%=:\n\t" \
  "s_waitcnt lgkmcnt(2)\n\t" \
  "v_mfma_f32_32x32x16_bf16 v[0:15], v[32:35], v[128:131], v[0:15]\n\t" \
  "s_add_u32 m0, %[w2k], 104448\n\t" \
  "s_nop 0\n\t" \
  "global_load_lds_dwordx4 v65, s[40:41] offset:1024\n\t" \
  "s_add_u32 s40, s40, 128\n\t" \
  "s_addc_u32 s41, s41, 0\n\t" \
  "ds_read_b128 v[136:139], v70 offset:0\n\t" \
  "ds_read_b128 v[32:35], v72 offset:320\n\t" \
  "s_waitcnt lgkmcnt(2)\n\t" \
  "v_mfma_f32_32x32x16_bf16 v[0:15], v[40:43], v[132:135], v[0:15]\n\t" \
  "ds_read_b128 v[140:143], v71 offset:0\n\t" \
  "ds_read_b128 v[40:43], v72 offset:352\n\t" \
  "s_branch Ea12_%=\n\t" \
  "Sa121_%=:\n\t" \
  "s_waitcnt lgkmcnt(5)\n\t" \
  "v_mfma_f32_32x32x16_bf16 v[0:15], v[128:131], v[32:35], v[0:15]\n\t" \
  "v_mfma_f32_32x32x16_bf16 v[16:31], v[128:131], v[36:39], v[16:31]\n\t" \
  "s_add_u32 m0, %[w2k], 104448\n\t" \
  "s_nop 0\n\t" \
  "global_load_lds_dwordx4 v65, s[40:41] offset:1024\n\t" \
  "s_add_u32 s40, s40, 128\n\t" \
  "s_addc_u32 s41, s41, 0\n\t" \
  "ds_read_b128 v[136:139], v70 offset:0\n\t" \
  "ds_read_b64_tr_b16 v[32:33], v72 offset:8192\n\t" \
  "ds_read_b64_tr_b16 v[34:35], v72 offset:9216\n\t" \
  "ds_read_b64_tr_b16 v[36:37], v73 offset:8192\n\t" \
  "ds_read_b64_tr_b16 v[38:39], v73 offset:9216\n\t" \
  "s_waitcnt lgkmcnt(5)\n\t" \
  "v_mfma_f32_32x32x16_bf16 v[0:15], v[132:135], v[40:43], v[0:15]\n\t" \
  "v_mfma_f32_32x32x16_bf16 v[16:31], v[132:135], v[44:47], v[16:31]\n\t" \
  "s_add_u32 m0, %[w2k], 120832\n\t" \
  "s_nop 0\n\t" \
  "global_load_lds_dwordx4 v66, s[42:43]\n\t" \
  "ds_read_b128 v[140:143], v71 offset:0\n\t" \
  "ds_read_b64_tr_b16 v[40:41], v72 offset:12288\n\t" \
  "ds_read_b64_tr_b16 v[42:43], v72 offset:13312\n\t" \
  "ds_read_b64_tr_b16 v[44:45], v73 offset:12288\n\t" \
  "ds_read_b64_tr_b16 v[46:47], v73 offset:13312\n\t" \
  "Ea12_%=:\n\t"
#define A1d \
  "s_nop 4\n\t" \
  "s_bitcmp1_b32 %[fl], 4\n\t" \
  "s_cbranch_scc1 Sa130_%=\n\t" \
  "s_bitcmp1_b32 %[fl], 3\n\t" \
  "s_cbranch_scc1 Sa131_%=\n\t" \
  "s_waitcnt lgkmcnt(5)\n\t" \
  "v_mfma_f32_32x32x16_bf16 v[0:15], v[32:35], v[136:139], v[0:15]\n\t" \
  "v_mfma_f32_32x32x16_bf16 v[16:31], v[36:39], v[136:139], v[16:31]\n\t" \
  "s_add_u32 m0, %[w2k], 120832\n\t" \
  "s_nop 0\n\t" \
  "global_load_lds_dwordx4 v67, s[42:43] offset:1024\n\t" \
  "s_add_u32 s42, s42, 245760\n\t" \
  "s_addc_u32 s43, s43, 0\n\t" \
  "s_waitcnt lgkmcnt(0)\n\t" \
  "s_waitcnt vmcnt(8)\n\t" \
  "s_barrier\n\t" \
  "ds_read_b128 v[144:147], v68 offset:34816\n\t" \
  "ds_read_b64_tr_b16 v[32:33], v72 offset:34816\n\t" \
  "ds_read_b64_tr_b16 v[34:35], v72 offset:35840\n\t" \
  "ds_read_b64_tr_b16 v[36:37], v73 offset:34816\n\t" \
  "ds_read_b64_tr_b16 v[38:39], v73 offset:35840\n\t" \
  "v_mfma_f32_32x32x16_bf16 v[0:15], v[40:43], v[140:143], v[0:15]\n\t" \
  "v_mfma_f32_32x32x16_bf16 v[16:31], v[44:47], v[140:143], v[16:31]\n\t" \
  "s_bitcmp1_b32 %[fl], 1\n\t" \
  "s_cbranch_scc0 La105_%=\n\t" \
  "v_add_u32 v89, 1, v89\n\t" \
  "global_store_dword v87, v89, %[fb] sc1\n\t" \
  "La105_%=:\n\t" \
  "s_add_u32 m0, %[w2k], 0\n\t" \
  "s_nop 0\n\t" \
  "global_load_lds_dwordx4 v64, s[40:41]\n\t" \
  "ds_read_b128 v[148:151], v69 offset:34816\n\t" \
  "ds_read_b64_tr_b16 v[40:41], v72 offset:38912\n\t" \
  "ds_read_b64_tr_b16 v[42:43], v72 offset:39936\n\t" \
  "ds_read_b64_tr_b16 v[44:45], v73 offset:38912\n\t" \
  "ds_read_b64_tr_b16 v[46:47], v73 offset:39936\n\t" \
  "s_waitcnt lgkmcnt(5)\n\t" \
  "v_mfma_f32_32x32x16_bf16 v[0:15], v[32:35], v[144:147], v[0:15]\n\t" \
  "v_mfma_f32_32x32x16_bf16 v[16:31], v[36:39], v[144:147], v[16:31]\n\t" \
  "global_load_lds_dwordx4 v65, s[40:41] offset:1024\n\t" \
  "s_add_u32 s40, s40, 128\n\t" \
  "s_addc_u32 s41, s41, 0\n\t" \
  "ds_read_b128 v[152:155], v70 offset:34816\n\t" \
  "ds_read_b64_tr_b16 v[32:33], v72 offset:43008\n\t" \
  "ds_read_b64_tr_b16 v[34:35], v72 offset:44032\n\t" \
  "ds_read_b64_tr_b16 v[36:37], v73 offset:43008\n\t" \
  "ds_read_b64_tr_b16 v[38:39], v73 offset:44032\n\t" \
  "s_branch Ea13_%=\n\t" \
  "Sa130_%=:\n\t" \
  "s_waitcnt lgkmcnt(2)\n\t" \
  "v_mfma_f32_32x32x16_bf16 v[0:15], v[32:35], v[136:139], v[0:15]\n\t" \
  "s_waitcnt lgkmcnt(0)\n\t" \
  "s_waitcnt vmcnt(4)\n\t" \
  "s_barrier\n\t" \
  "ds_read_b128 v[144:147], v68 offset:34816\n\t" \
  "ds_read_b128 v[32:35], v72 offset:384\n\t" \
  "v_mfma_f32_32x32x16_bf16 v[0:15], v[40:43], v[140:143], v[0:15]\n\t" \
  "s_bitcmp1_b32 %[fl], 1\n\t" \
  "s_cbranch_scc0 La125_%=\n\t" \
  "v_add_u32 v89, 1, v89\n\t" \
  "global_store_dword v87, v89, %[fb] sc1\n\t" \
  "La125_%=:\n\t" \
  "s_add_u32 m0, %[w2k], 0\n\t" \
  "s_nop 0\n\t" \
  "global_load_lds_dwordx4 v64, s[40:41]\n\t" \
  "ds_read_b128 v[148:151], v69 offset:34816\n\t" \
  "ds_read_b128 v[40:43], v72 offset:416\n\t" \
  "s_waitcnt lgkmcnt(2)\n\t" \
  "v_mfma_f32_32x32x16_bf16 v[0:15], v[32:35], v[144:147], v[0:15]\n\t" \
  "global_load_lds_dwordx4 v65, s[40:41] offset:1024\n\t" \
  "s_add_u32 s40, s40, 128\n\t" \
  "s_addc_u32 s41, s41, 0\n\t" \
  "ds_read_b128 v[152:155], v70 offset:34816\n\t" \
  "ds_read_b128 v[32:35], v72 offset:448\n\t" \
  "s_branch Ea13_%=\n\t" \
  "Sa131_%=:\n\t" \
  "s_waitcnt lgkmcnt(5)\n\t" \
  "v_mfma_f32_32x32x16_bf16 v[0:15], v[136:139], v[32:35], v[0:15]\n\t" \
  "v_mfma_f32_32x32x16_bf16 v[16:31], v[136:139], v[36:39], v[16:31]\n\t" \
  "s_add_u32 m0, %[w2k], 120832\n\t" \
  "s_nop 0\n\t" \
  "global_load_lds_dwordx4 v67, s[42:43] offset:1024\n\t" \
  "s_add_u32 s42, s42, 245760\n\t" \
  "s_addc_u32 s43, s43, 0\n\t" \
  "s_waitcnt lgkmcnt(0)\n\t" \
  "s_waitcnt vmcnt(8)\n\t" \
  "s_barrier\n\t" \
  "ds_read_b128 v[144:147], v68 offset:34816\n\t" \
  "ds_read_b64_tr_b16 v[32:33], v72 offset:34816\n\t" \
  "ds_read_b64_tr_b16 v[34:35], v72 offset:35840\n\t" \
  "ds_read_b64_tr_b16 v[36:37], v73 offset:34816\n\t" \
  "ds_read_b64_tr_b16 v[38:39], v73 offset:35840\n\t" \
  "v_mfma_f32_32x32x16_bf16 v[0:15], v[140:143], v[40:43], v[0:15]\n\t" \
  "v_mfma_f32_32x32x16_bf16 v[16:31], v[140:143], v[44:47], v[16:31]\n\t" \
  "s_bitcmp1_b32 %[fl], 1\n\t" \
  "s_cbranch_scc0 La115_%=\n\t" \
  "v_add_u32 v89, 1, v89\n\t" \
  "global_store_dword v87, v89, %[fb] sc1\n\t" \
  "La115_%=:\n\t" \
  "s_add_u32 m0, %[w2k], 0\n\t" \
  "s_nop 0\n\t" \
  "global_load_lds_dwordx4 v64, s[40:41]\n\t" \
  "ds_read_b128 v[148:151], v69 offset:34816\n\t" \
  "ds_read_b64_tr_b16 v[40:41], v72 offset:38912\n\t" \
  "ds_read_b64_tr_b16 v[42:43], v72 offset:39936\n\t" \
  "ds_read_b64_tr_b16 v[44:45], v73 offset:38912\n\t" \
  "ds_read_b64_tr_b16 v[46:47], v73 offset:39936\n\t" \
  "s_waitcnt lgkmcnt(5)\n\t" \
  "v_mfma_f32_32x32x16_bf16 v[0:15], v[144:147], v[32:35], v[0:15]\n\t" \
  "v_mfma_f32_32x32x16_bf16 v[16:31], v[144:147], v[36:39], v[16:31]\n\t" \
  "global_load_lds_dwordx4 v65, s[40:41] offset:1024\n\t" \
  "s_add_u32 s40, s40, 128\n\t" \
  "s_addc_u32 s41, s41, 0\n\t" \
  "ds_read_b128 v[152:155], v70 offset:34816\n\t" \
  "ds_read_b64_tr_b16 v[32:33], v72 offset:43008\n\t" \
  "ds_read_b64_tr_b16 v[34:35], v72 offset:44032\n\t" \
  "ds_read_b64_tr_b16 v[36:37], v73 offset:43008\n\t" \
  "ds_read_b64_tr_b16 v[38:39], v73 offset:44032\n\t" \
  "Ea13_%=:\n\t"
#define A1e \
  "s_nop 4\n\t" \
  "s_bitcmp1_b32 %[fl], 4\n\t" \
  "s_cbranch_scc1 Sa140_%=\n\t" \
  "s_bitcmp1_b32 %[fl], 3\n\t" \
  "s_cbranch_scc1 Sa141_%=\n\t" \
  "s_waitcnt lgkmcnt(5)\n\t" \
  "v_mfma_f32_32x32x16_bf16 v[0:15], v[40:43], v[148:151], v[0:15]\n\t" \
  "v_mfma_f32_32x32x16_bf16 v[16:31], v[44:47], v[148:151], v[16:31]\n\t" \
  "s_add_u32 m0, %[w2k], 16384\n\t" \
  "s_nop 0\n\t" \
  "global_load_lds_dwordx4 v66, s[42:43]\n\t" \
  "ds_read_b128 v[156:159], v71 offset:34816\n\t" \
  "ds_read_b64_tr_b16 v[40:41], v72 offset:47104\n\t" \
  "ds_read_b64_tr_b16 v[42:43], v72 offset:48128\n\t" \
  "ds_read_b64_tr_b16 v[44:45], v73 offset:47104\n\t" \
  "ds_read_b64_tr_b16 v[46:47], v73 offset:48128\n\t" \
  "s_waitcnt lgkmcnt(5)\n\t" \
  "v_mfma_f32_32x32x16_bf16 v[0:15], v[32:35], v[152:155], v[0:15]\n\t" \
  "v_mfma_f32_32x32x16_bf16 v[16:31], v[36:39], v[152:155], v[16:31]\n\t" \
  "global_load_lds_dwordx4 v67, s[42:43] offset:1024\n\t" \
  "s_add_u32 s42, s42, 245760\n\t" \
  "s_addc_u32 s43, s43, 0\n\t" \
  "s_add_u32 s46, s44, 1179648\n\t" \
  "s_addc_u32 s47, s45, 0\n\t" \
  "v_mov_b32 v91, 0\n\t" \
  "v_mov_b32 v92, 0\n\t" \
  "v_mov_b32 v93, 0\n\t" \
  "v_mov_b32 v95, 0\n\t" \
  "v_lshrrev_b32 v252, 5, v75\n\t" \
  "v_min_u32 v252, 9, v252\n\t" \
  "v_mul_u32_u24 v252, 3072, v252\n\t" \
  "v_and_b32 v253, 16, v75\n\t" \
  "v_lshl_add_u32 v253, v253, 3, s76\n\t" \
  "v_add_u32 v252, v252, v253\n\t" \
  "global_load_dword v252, v252, s[44:45]\n\t" \
  "s_waitcnt lgkmcnt(0)\n\t" \
  "s_waitcnt vmcnt(9)\n\t" \
  "s_barrier\n\t" \
  "v_add_u32 v68, 69632, v68\n\t" \
  "v_add_u32 v69, 69632, v69\n\t" \
  "v_add_u32 v70, 69632, v70\n\t" \
  "v_add_u32 v71, 69632, v71\n\t" \
  "v_add_u32 v72, 69632, v72\n\t" \
  "v_add_u32 v73, 69632, v73\n\t" \
  "ds_read_b128 v[160:163], v68 offset:0\n\t" \
  "ds_read_b64_tr_b16 v[32:33], v72 offset:0\n\t" \
  "ds_read_b64_tr_b16 v[34:35], v72 offset:1024\n\t" \
  "ds_read_b64_tr_b16 v[36:37], v73 offset:0\n\t" \
  "ds_read_b64_tr_b16 v[38:39], v73 offset:1024\n\t" \
  "v_mfma_f32_32x32x16_bf16 v[0:15], v[40:43], v[156:159], v[0:15]\n\t" \
  "v_mfma_f32_32x32x16_bf16 v[16:31], v[44:47], v[156:159], v[16:31]\n\t" \
  "s_bitcmp1_b32 %[fl], 1\n\t" \
  "s_cbranch_scc0 La106_%=\n\t" \
  "v_add_u32 v89, 1, v89\n\t" \
  "global_store_dword v87, v89, %[fb] sc1\n\t" \
  "La106_%=:\n\t" \
  "s_add_u32 m0, %[w2k], 34816\n\t" \
  "s_nop 0\n\t" \
  "global_load_lds_dwordx4 v64, s[40:41]\n\t" \
  "ds_read_b128 v[164:167], v69 offset:0\n\t" \
  "ds_read_b64_tr_b16 v[40:41], v72 offset:4096\n\t" \
  "ds_read_b64_tr_b16 v[42:43], v72 offset:5120\n\t" \
  "ds_read_b64_tr_b16 v[44:45], v73 offset:4096\n\t" \
  "ds_read_b64_tr_b16 v[46:47], v73 offset:5120\n\t" \
  "s_waitcnt lgkmcnt(5)\n\t" \
  "v_mfma_f32_32x32x16_bf16 v[0:15], v[32:35], v[160:163], v[0:15]\n\t" \
  "v_mfma_f32_32x32x16_bf16 v[16:31], v[36:39], v[160:163], v[16:31]\n\t" \
  "global_load_lds_dwordx4 v65, s[40:41] offset:1024\n\t" \
  "s_add_u32 s40, s40, 128\n\t" \
  "s_addc_u32 s41, s41, 0\n\t" \
  "global_load_ushort v90, v83, %[b1]\n\t" \
  "ds_read_b128 v[168:171], v70 offset:0\n\t" \
  "ds_read_b64_tr_b16 v[32:33], v72 offset:8192\n\t" \
  "ds_read_b64_tr_b16 v[34:35], v72 offset:9216\n\t" \
  "ds_read_b64_tr_b16 v[36:37], v73 offset:8192\n\t" \
  "ds_read_b64_tr_b16 v[38:39], v73 offset:9216\n\t" \
  "s_waitcnt lgkmcnt(5)\n\t" \
  "v_mfma_f32_32x32x16_bf16 v[0:15], v[40:43], v[164:167], v[0:15]\n\t" \
  "v_mfma_f32_32x32x16_bf16 v[16:31], v[44:47], v[164:167], v[16:31]\n\t" \
  "s_add_u32 m0, %[w2k], 51200\n\t" \
  "s_nop 0\n\t" \
  "global_load_lds_dwordx4 v66, s[42:43]\n\t" \
  "global_load_ushort v94, v83, %[b1] offset:64\n\t" \
  "ds_read_b128 v[172:175], v71 offset:0\n\t" \
  "ds_read_b64_tr_b16 v[40:41], v72 offset:12288\n\t" \
  "ds_read_b64_tr_b16 v[42:43], v72 offset:13312\n\t" \
  "ds_read_b64_tr_b16 v[44:45], v73 offset:12288\n\t" \
  "ds_read_b64_tr_b16 v[46:47], v73 offset:13312\n\t" \
  "s_waitcnt lgkmcnt(5)\n\t" \
  "v_mfma_f32_32x32x16_bf16 v[0:15], v[32:35], v[168:171], v[0:15]\n\t" \
  "v_mfma_f32_32x32x16_bf16 v[16:31], v[36:39], v[168:171], v[16:31]\n\t" \
  "global_load_lds_dwordx4 v67, s[42:43] offset:1024\n\t" \
  "s_add_u32 s42, s42, 245760\n\t" \
  "s_addc_u32 s43, s43, 0\n\t" \
  "s_waitcnt lgkmcnt(0)\n\t" \
  "s_waitcnt vmcnt(11)\n\t" \
  "s_barrier\n\t" \
  "ds_read_b128 v[176:179], v68 offset:34816\n\t" \
  "ds_read_b64_tr_b16 v[32:33], v72 offset:34816\n\t" \
  "ds_read_b64_tr_b16 v[34:35], v72 offset:35840\n\t" \
  "ds_read_b64_tr_b16 v[36:37], v73 offset:34816\n\t" \
  "ds_read_b64_tr_b16 v[38:39], v73 offset:35840\n\t" \
  "v_mfma_f32_32x32x16_bf16 v[0:15], v[40:43], v[172:175], v[0:15]\n\t" \
  "v_mfma_f32_32x32x16_bf16 v[16:31], v[44:47], v[172:175], v[16:31]\n\t" \
  "s_bitcmp1_b32 %[fl], 1\n\t" \
  "s_cbranch_scc0 La107_%=\n\t" \
  "v_add_u32 v89, 1, v89\n\t" \
  "global_store_dword v87, v89, %[fb] sc1\n\t" \
  "La107_%=:\n\t" \
  "s_add_u32 m0, %[w2k], 69632\n\t" \
  "s_nop 0\n\t" \
  "global_load_lds_dwordx4 v64, s[40:41]\n\t" \
  "ds_read_b128 v[180:183], v69 offset:34816\n\t" \
  "ds_read_b64_tr_b16 v[40:41], v72 offset:38912\n\t" \
  "ds_read_b64_tr_b16 v[42:43], v72 offset:39936\n\t" \
  "ds_read_b64_tr_b16 v[44:45], v73 offset:38912\n\t" \
  "ds_read_b64_tr_b16 v[46:47], v73 offset:39936\n\t" \
  "s_waitcnt lgkmcnt(5)\n\t" \
  "v_mfma_f32_32x32x16_bf16 v[0:15], v[32:35], v[176:179], v[0:15]\n\t" \
  "v_mfma_f32_32x32x16_bf16 v[16:31], v[36:39], v[176:179], v[16:31]\n\t" \
  "global_load_lds_dwordx4 v65, s[40:41] offset:1024\n\t" \
  "s_add_u32 s40, s40, 128\n\t" \
  "s_addc_u32 s41, s41, 0\n\t" \
  "ds_read_b128 v[184:187], v70 offset:34816\n\t" \
  "ds_read_b64_tr_b16 v[32:33], v72 offset:43008\n\t" \
  "ds_read_b64_tr_b16 v[34:35], v72 offset:44032\n\t" \
  "ds_read_b64_tr_b16 v[36:37], v73 offset:43008\n\t" \
  "ds_read_b64_tr_b16 v[38:39], v73 offset:44032\n\t" \
  "s_waitcnt lgkmcnt(5)\n\t" \
  "v_mfma_f32_32x32x16_bf16 v[0:15], v[40:43], v[180:183], v[0:15]\n\t" \
  "v_mfma_f32_32x32x16_bf16 v[16:31], v[44:47], v[180:183], v[16:31]\n\t" \
  "s_add_u32 m0, %[w2k], 86016\n\t" \
  "s_nop 0\n\t" \
  "global_load_lds_dwordx4 v66, s[42:43]\n\t" \
  "ds_read_b128 v[188:191], v71 offset:34816\n\t" \
  "ds_read_b64_tr_b16 v[40:41], v72 offset:47104\n\t" \
  "ds_read_b64_tr_b16 v[42:43], v72 offset:48128\n\t" \
  "ds_read_b64_tr_b16 v[44:45], v73 offset:47104\n\t" \
  "ds_read_b64_tr_b16 v[46:47], v73 offset:48128\n\t" \
  "s_waitcnt lgkmcnt(5)\n\t" \
  "v_mfma_f32_32x32x16_bf16 v[0:15], v[32:35], v[184:187], v[0:15]\n\t" \
  "v_mfma_f32_32x32x16_bf16 v[16:31], v[36:39], v[184:187], v[16:31]\n\t" \
  "global_load_lds_dwordx4 v67, s[42:43] offset:1024\n\t" \
  "s_add_u32 s42, s42, 245760\n\t" \
  "s_addc_u32 s43, s43, 0\n\t" \
  "s_waitcnt lgkmcnt(0)\n\t" \
  "s_waitcnt vmcnt(11)\n\t" \
  "s_barrier\n\t" \
  "v_add_u32 v68, -69632, v68\n\t" \
  "v_add_u32 v69, -69632, v69\n\t" \
  "v_add_u32 v70, -69632, v70\n\t" \
  "v_add_u32 v71, -69632, v71\n\t" \
  "v_add_u32 v72, -69632, v72\n\t" \
  "v_add_u32 v73, -69632, v73\n\t" \
  "ds_read_b128 v[192:195], v68 offset:0\n\t" \
  "ds_read_b64_tr_b16 v[32:33], v72 offset:0\n\t" \
  "ds_read_b64_tr_b16 v[34:35], v72 offset:1024\n\t" \
  "ds_read_b64_tr_b16 v[36:37], v73 offset:0\n\t" \
  "ds_read_b64_tr_b16 v[38:39], v73 offset:1024\n\t" \
  "v_mfma_f32_32x32x16_bf16 v[0:15], v[40:43], v[188:191], v[0:15]\n\t" \
  "v_mfma_f32_32x32x16_bf16 v[16:31], v[44:47], v[188:191], v[16:31]\n\t" \
  "s_bitcmp1_b32 %[fl], 1\n\t" \
  "s_cbranch_scc0 La108_%=\n\t" \
  "v_add_u32 v89, 1, v89\n\t" \
  "global_store_dword v87, v89, %[fb] sc1\n\t" \
  "La108_%=:\n\t" \
  "s_add_u32 m0, %[w2k], 104448\n\t" \
  "s_nop 0\n\t" \
  "global_load_lds_dwordx4 v64, s[40:41]\n\t" \
  "ds_read_b128 v[196:199], v69 offset:0\n\t" \
  "ds_read_b64_tr_b16 v[40:41], v72 offset:4096\n\t" \
  "ds_read_b64_tr_b16 v[42:43], v72 offset:5120\n\t" \
  "ds_read_b64_tr_b16 v[44:45], v73 offset:4096\n\t" \
  "ds_read_b64_tr_b16 v[46:47], v73 offset:5120\n\t" \
  "s_waitcnt lgkmcnt(5)\n\t" \
  "v_mfma_f32_32x32x16_bf16 v[0:15], v[32:35], v[192:195], v[0:15]\n\t" \
  "v_mfma_f32_32x32x16_bf16 v[16:31], v[36:39], v[192:195], v[16:31]\n\t" \
  "global_load_lds_dwordx4 v65, s[40:41] offset:1024\n\t" \
  "s_add_u32 s40, s40, 128\n\t" \
  "s_addc_u32 s41, s41, 0\n\t" \
  "ds_read_b128 v[200:203], v70 offset:0\n\t" \
  "ds_read_b64_tr_b16 v[32:33], v72 offset:8192\n\t" \
  "ds_read_b64_tr_b16 v[34:35], v72 offset:9216\n\t" \
  "ds_read_b64_tr_b16 v[36:37], v73 offset:8192\n\t" \
  "ds_read_b64_tr_b16 v[38:39], v73 offset:9216\n\t" \
  "s_waitcnt lgkmcnt(5)\n\t" \
  "v_mfma_f32_32x32x16_bf16 v[0:15], v[40:43], v[196:199], v[0:15]\n\t" \
  "v_mfma_f32_32x32x16_bf16 v[16:31], v[44:47], v[196:199], v[16:31]\n\t" \
  "s_add_u32 m0, %[w2k], 120832\n\t" \
  "s_nop 0\n\t" \
  "global_load_lds_dwordx4 v66, s[42:43]\n\t" \
  "ds_read_b128 v[204:207], v71 offset:0\n\t" \
  "ds_read_b64_tr_b16 v[40:41], v72 offset:12288\n\t" \
  "ds_read_b64_tr_b16 v[42:43], v72 offset:13312\n\t" \
  "ds_read_b64_tr_b16 v[44:45], v73 offset:12288\n\t" \
  "ds_read_b64_tr_b16 v[46:47], v73 offset:13312\n\t" \
  "s_waitcnt lgkmcnt(5)\n\t" \
  "v_mfma_f32_32x32x16_bf16 v[0:15], v[32:35], v[200:203], v[0:15]\n\t" \
  "v_mfma_f32_32x32x16_bf16 v[16:31], v[36:39], v[200:203], v[16:31]\n\t" \
  "global_load_lds_dwordx4 v67, s[42:43] offset:1024\n\t" \
  "s_add_u32 s42, s42, 245760\n\t" \
  "s_addc_u32 s43, s43, 0\n\t" \
  "s_waitcnt lgkmcnt(0)\n\t" \
  "s_waitcnt vmcnt(8)\n\t" \
  "s_barrier\n\t" \
  "ds_read_b128 v[208:211], v68 offset:34816\n\t" \
  "ds_read_b64_tr_b16 v[32:33], v72 offset:34816\n\t" \
  "ds_read_b64_tr_b16 v[34:35], v72 offset:35840\n\t" \
  "ds_read_b64_tr_b16 v[36:37], v73 offset:34816\n\t" \
  "ds_read_b64_tr_b16 v[38:39], v73 offset:35840\n\t" \
  "v_mfma_f32_32x32x16_bf16 v[0:15], v[40:43], v[204:207], v[0:15]\n\t" \
  "v_mfma_f32_32x32x16_bf16 v[16:31], v[44:47], v[204:207], v[16:31]\n\t" \
  "s_bitcmp1_b32 %[fl], 1\n\t" \
  "s_cbranch_scc0 La109_%=\n\t" \
  "v_add_u32 v89, 1, v89\n\t" \
  "global_store_dword v87, v89, %[fb] sc1\n\t" \
  "La109_%=:\n\t" \
  "s_add_u32 m0, %[w3k], 0\n\t" \
  "s_nop 0\n\t" \
  "global_load_lds_dwordx4 v80, s[44:45]\n\t" \
  "ds_read_b128 v[212:215], v69 offset:34816\n\t" \
  "ds_read_b64_tr_b16 v[40:41], v72 offset:38912\n\t" \
  "ds_read_b64_tr_b16 v[42:43], v72 offset:39936\n\t" \
  "ds_read_b64_tr_b16 v[44:45], v73 offset:38912\n\t" \
  "ds_read_b64_tr_b16 v[46:47], v73 offset:39936\n\t" \
  "s_waitcnt lgkmcnt(5)\n\t" \
  "v_mfma_f32_32x32x16_bf16 v[0:15], v[32:35], v[208:211], v[0:15]\n\t" \
  "v_mfma_f32_32x32x16_bf16 v[16:31], v[36:39], v[208:211], v[16:31]\n\t" \
  "global_load_lds_dwordx4 v81, s[44:45] offset:1024\n\t" \
  "ds_read_b128 v[216:219], v70 offset:34816\n\t" \
  "ds_read_b64_tr_b16 v[32:33], v72 offset:43008\n\t" \
  "ds_read_b64_tr_b16 v[34:35], v72 offset:44032\n\t" \
  "ds_read_b64_tr_b16 v[36:37], v73 offset:43008\n\t" \
  "ds_read_b64_tr_b16 v[38:39], v73 offset:44032\n\t" \
  "s_waitcnt lgkmcnt(5)\n\t" \
  "v_mfma_f32_32x32x16_bf16 v[0:15], v[40:43], v[212:215], v[0:15]\n\t" \
  "v_mfma_f32_32x32x16_bf16 v[16:31], v[44:47], v[212:215], v[16:31]\n\t" \
  "global_load_lds_dwordx4 v82, s[44:45] offset:2048\n\t" \
  "s_add_u32 s44, s44, 393216\n\t" \
  "s_addc_u32 s45, s45, 0\n\t" \
  "ds_read_b128 v[220:223], v71 offset:34816\n\t" \
  "ds_read_b64_tr_b16 v[40:41], v72 offset:47104\n\t" \
  "ds_read_b64_tr_b16 v[42:43], v72 offset:48128\n\t" \
  "ds_read_b64_tr_b16 v[44:45], v73 offset:47104\n\t" \
  "ds_read_b64_tr_b16 v[46:47], v73 offset:48128\n\t" \
  "s_waitcnt lgkmcnt(5)\n\t" \
  "v_mfma_f32_32x32x16_bf16 v[0:15], v[32:35], v[216:219], v[0:15]\n\t" \
  "v_mfma_f32_32x32x16_bf16 v[16:31], v[36:39], v[216:219], v[16:31]\n\t" \
  "s_add_u32 m0, %[w3k], 139264\n\t" \
  "s_nop 0\n\t" \
  "global_load_lds_dwordx4 v80, s[46:47]\n\t" \
  "v_lshrrev_b32 v64, 9, v75\n\t" \
  "v_mov_b32 v65, 0\n\t" \
  "v_mov_b32 v67, 0\n\t" \
  "v_mul_u32_u24 v64, 0x3f80, v64\n\t" \
  "v_mov_b32 v66, v64\n\t" \
  "s_waitcnt lgkmcnt(0)\n\t" \
  "s_waitcnt vmcnt(8)\n\t" \
  "s_barrier\n\t" \
  "v_add_u32 v68, 69632, v68\n\t" \
  "v_add_u32 v69, 69632, v69\n\t" \
  "v_add_u32 v70, 69632, v70\n\t" \
  "v_add_u32 v71, 69632, v71\n\t" \
  "v_add_u32 v72, 69632, v72\n\t" \
  "v_add_u32 v73, 69632, v73\n\t" \
  "ds_read_b128 v[224:227], v68 offset:0\n\t" \
  "ds_read_b64_tr_b16 v[32:33], v72 offset:0\n\t" \
  "ds_read_b64_tr_b16 v[34:35], v72 offset:1024\n\t" \
  "ds_read_b64_tr_b16 v[36:37], v73 offset:0\n\t" \
  "ds_read_b64_tr_b16 v[38:39], v73 offset:1024\n\t" \
  "v_mfma_f32_32x32x16_bf16 v[0:15], v[40:43], v[220:223], v[0:15]\n\t" \
  "v_mfma_f32_32x32x16_bf16 v[16:31], v[44:47], v[220:223], v[16:31]\n\t" \
  "s_bitcmp1_b32 %[fl], 1\n\t" \
  "s_cbranch_scc0 La1010_%=\n\t" \
  "v_add_u32 v89, 1, v89\n\t" \
  "global_store_dword v87, v89, %[fb] sc1\n\t" \
  "La1010_%=:\n\t" \
  "s_add_u32 m0, %[w3k], 34816\n\t" \
  "s_nop 0\n\t" \
  "global_load_lds_dwordx4 v80, s[44:45]\n\t" \
  "ds_read_b128 v[228:231], v69 offset:0\n\t" \
  "ds_read_b64_tr_b16 v[40:41], v72 offset:4096\n\t" \
  "ds_read_b64_tr_b16 v[42:43], v72 offset:5120\n\t" \
  "ds_read_b64_tr_b16 v[44:45], v73 offset:4096\n\t" \
  "ds_read_b64_tr_b16 v[46:47], v73 offset:5120\n\t" \
  "s_waitcnt lgkmcnt(5)\n\t" \
  "v_mfma_f32_32x32x16_bf16 v[0:15], v[32:35], v[224:227], v[0:15]\n\t" \
  "v_mfma_f32_32x32x16_bf16 v[16:31], v[36:39], v[224:227], v[16:31]\n\t" \
  "global_load_lds_dwordx4 v81, s[44:45] offset:1024\n\t" \
  "ds_read_b128 v[232:235], v70 offset:0\n\t" \
  "ds_read_b64_tr_b16 v[32:33], v72 offset:8192\n\t" \
  "ds_read_b64_tr_b16 v[34:35], v72 offset:9216\n\t" \
  "ds_read_b64_tr_b16 v[36:37], v73 offset:8192\n\t" \
  "ds_read_b64_tr_b16 v[38:39], v73 offset:9216\n\t" \
  "s_waitcnt lgkmcnt(5)\n\t" \
  "v_mfma_f32_32x32x16_bf16 v[0:15], v[40:43], v[228:231], v[0:15]\n\t" \
  "v_mfma_f32_32x32x16_bf16 v[16:31], v[44:47], v[228:231], v[16:31]\n\t" \
  "global_load_lds_dwordx4 v82, s[44:45] offset:2048\n\t" \
  "s_add_u32 s44, s44, 393216\n\t" \
  "s_addc_u32 s45, s45, 0\n\t" \
  "ds_read_b128 v[236:239], v71 offset:0\n\t" \
  "ds_read_b64_tr_b16 v[40:41], v72 offset:12288\n\t" \
  "ds_read_b64_tr_b16 v[42:43], v72 offset:13312\n\t" \
  "ds_read_b64_tr_b16 v[44:45], v73 offset:12288\n\t" \
  "ds_read_b64_tr_b16 v[46:47], v73 offset:13312\n\t" \
  "s_waitcnt lgkmcnt(5)\n\t" \
  "v_mfma_f32_32x32x16_bf16 v[0:15], v[32:35], v[232:235], v[0:15]\n\t" \
  "v_mfma_f32_32x32x16_bf16 v[16:31], v[36:39], v[232:235], v[16:31]\n\t" \
  "s_add_u32 m0, %[w3k], 139264\n\t" \
  "s_nop 0\n\t" \
  "global_load_lds_dwordx4 v81, s[46:47] offset:1024\n\t" \
  "s_waitcnt lgkmcnt(0)\n\t" \
  "s_waitcnt vmcnt(8)\n\t" \
  "s_barrier\n\t" \
  "ds_read_b128 v[240:243], v68 offset:34816\n\t" \
  "ds_read_b64_tr_b16 v[32:33], v72 offset:34816\n\t" \
  "ds_read_b64_tr_b16 v[34:35], v72 offset:35840\n\t" \
  "ds_read_b64_tr_b16 v[36:37], v73 offset:34816\n\t" \
  "ds_read_b64_tr_b16 v[38:39], v73 offset:35840\n\t" \
  "v_mfma_f32_32x32x16_bf16 v[0:15], v[40:43], v[236:239], v[0:15]\n\t" \
  "v_mfma_f32_32x32x16_bf16 v[16:31], v[44:47], v[236:239], v[16:31]\n\t" \
  "s_bitcmp1_b32 %[fl], 1\n\t" \
  "s_cbranch_scc0 La1011_%=\n\t" \
  "v_add_u32 v89, 1, v89\n\t" \
  "global_store_dword v87, v89, %[fb] sc1\n\t" \
  "La1011_%=:\n\t" \
  "s_add_u32 m0, %[w3k], 69632\n\t" \
  "s_nop 0\n\t" \
  "global_load_lds_dwordx4 v80, s[44:45]\n\t" \
  "ds_read_b128 v[244:247], v69 offset:34816\n\t" \
  "ds_read_b64_tr_b16 v[40:41], v72 offset:38912\n\t" \
  "ds_read_b64_tr_b16 v[42:43], v72 offset:39936\n\t" \
  "ds_read_b64_tr_b16 v[44:45], v73 offset:38912\n\t" \
  "ds_read_b64_tr_b16 v[46:47], v73 offset:39936\n\t" \
  "s_waitcnt lgkmcnt(5)\n\t" \
  "v_mfma_f32_32x32x16_bf16 v[0:15], v[32:35], v[240:243], v[0:15]\n\t" \
  "v_mfma_f32_32x32x16_bf16 v[16:31], v[36:39], v[240:243], v[16:31]\n\t" \
  "global_load_lds_dwordx4 v81, s[44:45] offset:1024\n\t" \
  "ds_read_b128 v[248:251], v70 offset:34816\n\t" \
  "ds_read_b64_tr_b16 v[32:33], v72 offset:43008\n\t" \
  "ds_read_b64_tr_b16 v[34:35], v72 offset:44032\n\t" \
  "ds_read_b64_tr_b16 v[36:37], v73 offset:43008\n\t" \
  "ds_read_b64_tr_b16 v[38:39], v73 offset:44032\n\t" \
  "s_waitcnt lgkmcnt(5)\n\t" \
  "v_mfma_f32_32x32x16_bf16 v[0:15], v[40:43], v[244:247], v[0:15]\n\t" \
  "v_mfma_f32_32x32x16_bf16 v[16:31], v[44:47], v[244:247], v[16:31]\n\t" \
  "global_load_lds_dwordx4 v82, s[44:45] offset:2048\n\t" \
  "s_add_u32 s44, s44, 786432\n\t" \
  "s_addc_u32 s45, s45, 0\n\t" \
  "ds_read_b128 v[252:255], v71 offset:34816\n\t" \
  "ds_read_b64_tr_b16 v[40:41], v72 offset:47104\n\t" \
  "ds_read_b64_tr_b16 v[42:43], v72 offset:48128\n\t" \
  "ds_read_b64_tr_b16 v[44:45], v73 offset:47104\n\t" \
  "ds_read_b64_tr_b16 v[46:47], v73 offset:48128\n\t" \
  "s_waitcnt lgkmcnt(5)\n\t" \
  "v_mfma_f32_32x32x16_bf16 v[0:15], v[32:35], v[248:251], v[0:15]\n\t" \
  "v_mfma_f32_32x32x16_bf16 v[16:31], v[36:39], v[248:251], v[16:31]\n\t" \
  "s_add_u32 m0, %[w3k], 139264\n\t" \
  "s_nop 0\n\t" \
  "global_load_lds_dwordx4 v82, s[46:47] offset:2048\n\t" \
  "s_waitcnt lgkmcnt(0)\n\t" \
  "s_waitcnt vmcnt(9)\n\t" \
  "s_barrier\n\t" \
  "ds_read_b64_tr_b16 v[32:33], v74 offset:0\n\t" \
  "ds_read_b64_tr_b16 v[34:35], v74 offset:768\n\t" \
  "ds_read_b64_tr_b16 v[36:37], v74 offset:64\n\t" \
  "ds_read_b64_tr_b16 v[38:39], v74 offset:832\n\t" \
  "v_mfma_f32_32x32x16_bf16 v[0:15], v[40:43], v[252:255], v[0:15]\n\t" \
  "v_mfma_f32_32x32x16_bf16 v[16:31], v[44:47], v[252:255], v[16:31]\n\t" \
  "s_bitcmp1_b32 %[fl], 1\n\t" \
  "s_cbranch_scc0 La1012_%=\n\t" \
  "v_add_u32 v89, 1, v89\n\t" \
  "global_store_dword v87, v89, %[fb] sc1\n\t" \
  "La1012_%=:\n\t" \
  "s_nop 7\n\t" \
  "s_nop 3\n\t" \
  "s_bitcmp1_b32 %[fl], 16\n\t" \
  "s_cbranch_scc0 La1013_%=\n\t" \
  "s_mov_b32 s46, 0x3ea52b5c\n\t" \
  "v_pk_mul_f32 v[0:1], v[0:1], s[46:47] op_sel_hi:[1,0]\n\t" \
  "v_pk_mul_f32 v[2:3], v[2:3], s[46:47] op_sel_hi:[1,0]\n\t" \
  "v_pk_mul_f32 v[4:5], v[4:5], s[46:47] op_sel_hi:[1,0]\n\t" \
  "v_pk_mul_f32 v[6:7], v[6:7], s[46:47] op_sel_hi:[1,0]\n\t" \
  "v_pk_mul_f32 v[8:9], v[8:9], s[46:47] op_sel_hi:[1,0]\n\t" \
  "v_pk_mul_f32 v[10:11], v[10:11], s[46:47] op_sel_hi:[1,0]\n\t" \
  "v_pk_mul_f32 v[12:13], v[12:13], s[46:47] op_sel_hi:[1,0]\n\t" \
  "v_pk_mul_f32 v[14:15], v[14:15], s[46:47] op_sel_hi:[1,0]\n\t" \
  "v_pk_mul_f32 v[16:17], v[16:17], s[46:47] op_sel_hi:[1,0]\n\t" \
  "v_pk_mul_f32 v[18:19], v[18:19], s[46:47] op_sel_hi:[1,0]\n\t" \
  "v_pk_mul_f32 v[20:21], v[20:21], s[46:47] op_sel_hi:[1,0]\n\t" \
  "v_pk_mul_f32 v[22:23], v[22:23], s[46:47] op_sel_hi:[1,0]\n\t" \
  "v_pk_mul_f32 v[24:25], v[24:25], s[46:47] op_sel_hi:[1,0]\n\t" \
  "v_pk_mul_f32 v[26:27], v[26:27], s[46:47] op_sel_hi:[1,0]\n\t" \
  "v_pk_mul_f32 v[28:29], v[28:29], s[46:47] op_sel_hi:[1,0]\n\t" \
  "v_pk_mul_f32 v[30:31], v[30:31], s[46:47] op_sel_hi:[1,0]\n\t" \
  "La1013_%=:\n\t" \
  "v_cvt_pk_bf16_f32 v48, v0, v1\n\t" \
  "v_cvt_pk_bf16_f32 v49, v2, v3\n\t" \
  "v_cvt_pk_bf16_f32 v50, v4, v5\n\t" \
  "v_cvt_pk_bf16_f32 v51, v6, v7\n\t" \
  "v_cvt_pk_bf16_f32 v52, v8, v9\n\t" \
  "v_cvt_pk_bf16_f32 v53, v10, v11\n\t" \
  "v_cvt_pk_bf16_f32 v54, v12, v13\n\t" \
  "v_cvt_pk_bf16_f32 v55, v14, v15\n\t" \
  "v_cvt_pk_bf16_f32 v56, v16, v17\n\t" \
  "v_cvt_pk_bf16_f32 v57, v18, v19\n\t" \
  "v_cvt_pk_bf16_f32 v58, v20, v21\n\t" \
  "v_cvt_pk_bf16_f32 v59, v22, v23\n\t" \
  "v_cvt_pk_bf16_f32 v60, v24, v25\n\t" \
  "v_cvt_pk_bf16_f32 v61, v26, v27\n\t" \
  "v_cvt_pk_bf16_f32 v62, v28, v29\n\t" \
  "v_cvt_pk_bf16_f32 v63, v30, v31\n\t" \
  "s_branch Ea14_%=\n\t" \
  "Sa140_%=:\n\t" \
  "s_waitcnt lgkmcnt(2)\n\t" \
  "v_mfma_f32_32x32x16_bf16 v[0:15], v[40:43], v[148:151], v[0:15]\n\t" \
  "ds_read_b128 v[156:159], v71 offset:34816\n\t" \
  "ds_read_b128 v[40:43], v72 offset:480\n\t" \
  "s_waitcnt lgkmcnt(2)\n\t" \
  "v_mfma_f32_32x32x16_bf16 v[0:15], v[32:35], v[152:155], v[0:15]\n\t" \
  "s_add_u32 s46, s44, 1179648\n\t" \
  "s_addc_u32 s47, s45, 0\n\t" \
  "v_mov_b32 v91, 0\n\t" \
  "v_mov_b32 v92, 0\n\t" \
  "v_mov_b32 v93, 0\n\t" \
  "v_mov_b32 v95, 0\n\t" \
  "v_bfe_i32 v48, v75, 4, 1\n\t" \
  "v_not_b32 v48, v48\n\t" \
  "v_and_b32 v48, 0x3f803f80, v48\n\t" \
  "v_mov_b32 v49, v48\n\t" \
  "v_mov_b32 v50, v48\n\t" \
  "v_mov_b32 v51, v48\n\t" \
  "v_lshrrev_b32 v252, 5, v75\n\t" \
  "v_min_u32 v252, 9, v252\n\t" \
  "v_mul_u32_u24 v252, 3072, v252\n\t" \
  "v_and_b32 v253, 16, v75\n\t" \
  "v_lshl_add_u32 v253, v253, 3, s76\n\t" \
  "v_add_u32 v252, v252, v253\n\t" \
  "global_load_dword v252, v252, s[44:45]\n\t" \
  "s_waitcnt lgkmcnt(0)\n\t" \
  "s_waitcnt vmcnt(5)\n\t" \
  "s_barrier\n\t" \
  "v_add_u32 v68, 69632, v68\n\t" \
  "v_add_u32 v69, 69632, v69\n\t" \
  "v_add_u32 v70, 69632, v70\n\t" \
  "v_add_u32 v71, 69632, v71\n\t" \
  "ds_read_b128 v[160:163], v68 offset:0\n\t" \
  "ds_read_b128 v[32:35], v72 offset:512\n\t" \
  "v_mfma_f32_32x32x16_bf16 v[0:15], v[40:43], v[156:159], v[0:15]\n\t" \
  "s_bitcmp1_b32 %[fl], 1\n\t" \
  "s_cbranch_scc0 La126_%=\n\t" \
  "v_add_u32 v89, 1, v89\n\t" \
  "global_store_dword v87, v89, %[fb] sc1\n\t" \
  "La126_%=:\n\t" \
  "s_add_u32 m0, %[w2k], 34816\n\t" \
  "s_nop 0\n\t" \
  "global_load_lds_dwordx4 v64, s[40:41]\n\t" \
  "ds_read_b128 v[164:167], v69 offset:0\n\t" \
  "ds_read_b128 v[40:43], v72 offset:544\n\t" \
  "s_waitcnt lgkmcnt(2)\n\t" \
  "v_mfma_f32_32x32x16_bf16 v[0:15], v[32:35], v[160:163], v[0:15]\n\t" \
  "global_load_lds_dwordx4 v65, s[40:41] offset:1024\n\t" \
  "s_add_u32 s40, s40, 128\n\t" \
  "s_addc_u32 s41, s41, 0\n\t" \
  "global_load_ushort v90, v83, %[b1]\n\t" \
  "ds_read_b128 v[168:171], v70 offset:0\n\t" \
  "ds_read_b128 v[32:35], v72 offset:576\n\t" \
  "s_waitcnt lgkmcnt(2)\n\t" \
  "v_mfma_f32_32x32x16_bf16 v[0:15], v[40:43], v[164:167], v[0:15]\n\t" \
  "global_load_ushort v94, v83, %[b1] offset:64\n\t" \
  "ds_read_b128 v[172:175], v71 offset:0\n\t" \
  "ds_read_b128 v[40:43], v72 offset:608\n\t" \
  "s_waitcnt lgkmcnt(2)\n\t" \
  "v_mfma_f32_32x32x16_bf16 v[0:15], v[32:35], v[168:171], v[0:15]\n\t" \
  "s_waitcnt lgkmcnt(0)\n\t" \
  "s_waitcnt vmcnt(7)\n\t" \
  "s_barrier\n\t" \
  "ds_read_b128 v[176:179], v68 offset:34816\n\t" \
  "ds_read_b128 v[32:35], v72 offset:640\n\t" \
  "v_mfma_f32_32x32x16_bf16 v[0:15], v[40:43], v[172:175], v[0:15]\n\t" \
  "s_bitcmp1_b32 %[fl], 1\n\t" \
  "s_cbranch_scc0 La127_%=\n\t" \
  "v_add_u32 v89, 1, v89\n\t" \
  "global_store_dword v87, v89, %[fb] sc1\n\t" \
  "La127_%=:\n\t" \
  "s_add_u32 m0, %[w2k], 69632\n\t" \
  "s_nop 0\n\t" \
  "global_load_lds_dwordx4 v64, s[40:41]\n\t" \
  "ds_read_b128 v[180:183], v69 offset:34816\n\t" \
  "ds_read_b128 v[40:43], v72 offset:672\n\t" \
  "s_waitcnt lgkmcnt(2)\n\t" \
  "v_mfma_f32_32x32x16_bf16 v[0:15], v[32:35], v[176:179], v[0:15]\n\t" \
  "global_load_lds_dwordx4 v65, s[40:41] offset:1024\n\t" \
  "s_add_u32 s40, s40, 128\n\t" \
  "s_addc_u32 s41, s41, 0\n\t" \
  "ds_read_b128 v[184:187], v70 offset:34816\n\t" \
  "ds_read_b128 v[32:35], v72 offset:704\n\t" \
  "s_waitcnt lgkmcnt(2)\n\t" \
  "v_mfma_f32_32x32x16_bf16 v[0:15], v[40:43], v[180:183], v[0:15]\n\t" \
  "ds_read_b128 v[188:191], v71 offset:34816\n\t" \
  "ds_read_b128 v[40:43], v72 offset:736\n\t" \
  "s_waitcnt lgkmcnt(2)\n\t" \
  "v_mfma_f32_32x32x16_bf16 v[0:15], v[32:35], v[184:187], v[0:15]\n\t" \
  "s_waitcnt lgkmcnt(0)\n\t" \
  "s_waitcnt vmcnt(7)\n\t" \
  "s_barrier\n\t" \
  "v_add_u32 v68, -69632, v68\n\t" \
  "v_add_u32 v69, -69632, v69\n\t" \
  "v_add_u32 v70, -69632, v70\n\t" \
  "v_add_u32 v71, -69632, v71\n\t" \
  "ds_read_b128 v[192:195], v68 offset:0\n\t" \
  "ds_read_b128 v[32:35], v72 offset:768\n\t" \
  "v_mfma_f32_32x32x16_bf16 v[0:15], v[40:43], v[188:191], v[0:15]\n\t" \
  "s_bitcmp1_b32 %[fl], 1\n\t" \
  "s_cbranch_scc0 La128_%=\n\t" \
  "v_add_u32 v89, 1, v89\n\t" \
  "global_store_dword v87, v89, %[fb] sc1\n\t" \
  "La128_%=:\n\t" \
  "s_add_u32 m0, %[w2k], 104448\n\t" \
  "s_nop 0\n\t" \
  "global_load_lds_dwordx4 v64, s[40:41]\n\t" \
  "ds_read_b128 v[196:199], v69 offset:0\n\t" \
  "ds_read_b128 v[40:43], v72 offset:800\n\t" \
  "s_waitcnt lgkmcnt(2)\n\t" \
  "v_mfma_f32_32x32x16_bf16 v[0:15], v[32:35], v[192:195], v[0:15]\n\t" \
  "global_load_lds_dwordx4 v65, s[40:41] offset:1024\n\t" \
  "s_add_u32 s40, s40, 128\n\t" \
  "s_addc_u32 s41, s41, 0\n\t" \
  "ds_read_b128 v[200:203], v70 offset:0\n\t" \
  "ds_read_b128 v[32:35], v72 offset:832\n\t" \
  "s_waitcnt lgkmcnt(2)\n\t" \
  "v_mfma_f32_32x32x16_bf16 v[0:15], v[40:43], v[196:199], v[0:15]\n\t" \
  "ds_read_b128 v[204:207], v71 offset:0\n\t" \
  "ds_read_b128 v[40:43], v72 offset:864\n\t" \
  "s_waitcnt lgkmcnt(2)\n\t" \
  "v_mfma_f32_32x32x16_bf16 v[0:15], v[32:35], v[200:203], v[0:15]\n\t" \
  "s_waitcnt lgkmcnt(0)\n\t" \
  "s_waitcnt vmcnt(6)\n\t" \
  "s_barrier\n\t" \
  "ds_read_b128 v[208:211], v68 offset:34816\n\t" \
  "ds_read_b128 v[32:35], v72 offset:896\n\t" \
  "v_mfma_f32_32x32x16_bf16 v[0:15], v[40:43], v[204:207], v[0:15]\n\t" \
  "s_bitcmp1_b32 %[fl], 1\n\t" \
  "s_cbranch_scc0 La129_%=\n\t" \
  "v_add_u32 v89, 1, v89\n\t" \
  "global_store_dword v87, v89, %[fb] sc1\n\t" \
  "La129_%=:\n\t" \
  "s_add_u32 m0, %[w3k], 0\n\t" \
  "s_nop 0\n\t" \
  "global_load_lds_dwordx4 v80, s[44:45]\n\t" \
  "ds_read_b128 v[212:215], v69 offset:34816\n\t" \
  "ds_read_b128 v[40:43], v72 offset:928\n\t" \
  "s_waitcnt lgkmcnt(2)\n\t" \
  "v_mfma_f32_32x32x16_bf16 v[0:15], v[32:35], v[208:211], v[0:15]\n\t" \
  "global_load_lds_dwordx4 v81, s[44:45] offset:1024\n\t" \
  "ds_read_b128 v[216:219], v70 offset:34816\n\t" \
  "ds_read_b128 v[32:35], v72 offset:960\n\t" \
  "s_waitcnt lgkmcnt(2)\n\t" \
  "v_mfma_f32_32x32x16_bf16 v[0:15], v[40:43], v[212:215], v[0:15]\n\t" \
  "global_load_lds_dwordx4 v82, s[44:45] offset:2048\n\t" \
  "s_add_u32 s44, s44, 393216\n\t" \
  "s_addc_u32 s45, s45, 0\n\t" \
  "ds_read_b128 v[220:223], v71 offset:34816\n\t" \
  "ds_read_b128 v[40:43], v72 offset:992\n\t" \
  "s_waitcnt lgkmcnt(2)\n\t" \
  "v_mfma_f32_32x32x16_bf16 v[0:15], v[32:35], v[216:219], v[0:15]\n\t" \
  "s_add_u32 m0, %[w3k], 139264\n\t" \
  "s_nop 0\n\t" \
  "global_load_lds_dwordx4 v80, s[46:47]\n\t" \
  "v_lshrrev_b32 v64, 9, v75\n\t" \
  "v_mov_b32 v65, 0\n\t" \
  "v_mov_b32 v67, 0\n\t" \
  "v_mul_u32_u24 v64, 0x3f80, v64\n\t" \
  "v_mov_b32 v66, v64\n\t" \
  "s_waitcnt lgkmcnt(0)\n\t" \
  "s_waitcnt vmcnt(6)\n\t" \
  "s_barrier\n\t" \
  "v_add_u32 v68, 69632, v68\n\t" \
  "v_add_u32 v69, 69632, v69\n\t" \
  "v_add_u32 v70, 69632, v70\n\t" \
  "v_add_u32 v71, 69632, v71\n\t" \
  "ds_read_b128 v[224:227], v68 offset:0\n\t" \
  "ds_read_b128 v[32:35], v72 offset:1024\n\t" \
  "v_mfma_f32_32x32x16_bf16 v[0:15], v[40:43], v[220:223], v[0:15]\n\t" \
  "s_bitcmp1_b32 %[fl], 1\n\t" \
  "s_cbranch_scc0 La1210_%=\n\t" \
  "v_add_u32 v89, 1, v89\n\t" \
  "global_store_dword v87, v89, %[fb] sc1\n\t" \
  "La1210_%=:\n\t" \
  "s_add_u32 m0, %[w3k], 34816\n\t" \
  "s_nop 0\n\t" \
  "global_load_lds_dwordx4 v80, s[44:45]\n\t" \
  "ds_read_b128 v[228:231], v69 offset:0\n\t" \
  "ds_read_b128 v[40:43], v72 offset:1056\n\t" \
  "s_waitcnt lgkmcnt(2)\n\t" \
  "v_mfma_f32_32x32x16_bf16 v[0:15], v[32:35], v[224:227], v[0:15]\n\t" \
  "global_load_lds_dwordx4 v81, s[44:45] offset:1024\n\t" \
  "ds_read_b128 v[232:235], v70 offset:0\n\t" \
  "ds_read_b128 v[32:35], v72 offset:1088\n\t" \
  "s_waitcnt lgkmcnt(2)\n\t" \
  "v_mfma_f32_32x32x16_bf16 v[0:15], v[40:43], v[228:231], v[0:15]\n\t" \
  "global_load_lds_dwordx4 v82, s[44:45] offset:2048\n\t" \
  "s_add_u32 s44, s44, 393216\n\t" \
  "s_addc_u32 s45, s45, 0\n\t" \
  "ds_read_b128 v[236:239], v71 offset:0\n\t" \
  "ds_read_b128 v[40:43], v72 offset:1120\n\t" \
  "s_waitcnt lgkmcnt(2)\n\t" \
  "v_mfma_f32_32x32x16_bf16 v[0:15], v[32:35], v[232:235], v[0:15]\n\t" \
  "s_add_u32 m0, %[w3k], 139264\n\t" \
  "s_nop 0\n\t" \
  "global_load_lds_dwordx4 v81, s[46:47] offset:1024\n\t" \
  "s_waitcnt lgkmcnt(0)\n\t" \
  "s_waitcnt vmcnt(8)\n\t" \
  "s_barrier\n\t" \
  "ds_read_b128 v[240:243], v68 offset:34816\n\t" \
  "ds_read_b128 v[32:35], v72 offset:1152\n\t" \
  "v_mfma_f32_32x32x16_bf16 v[0:15], v[40:43], v[236:239], v[0:15]\n\t" \
  "s_bitcmp1_b32 %[fl], 1\n\t" \
  "s_cbranch_scc0 La1211_%=\n\t" \
  "v_add_u32 v89, 1, v89\n\t" \
  "global_store_dword v87, v89, %[fb] sc1\n\t" \
  "La1211_%=:\n\t" \
  "s_add_u32 m0, %[w3k], 69632\n\t" \
  "s_nop 0\n\t" \
  "global_load_lds_dwordx4 v80, s[44:45]\n\t" \
  "ds_read_b128 v[244:247], v69 offset:34816\n\t" \
  "ds_read_b128 v[40:43], v72 offset:1184\n\t" \
  "s_waitcnt lgkmcnt(2)\n\t" \
  "v_mfma_f32_32x32x16_bf16 v[0:15], v[32:35], v[240:243], v[0:15]\n\t" \
  "global_load_lds_dwordx4 v81, s[44:45] offset:1024\n\t" \
  "ds_read_b128 v[248:251], v70 offset:34816\n\t" \
  "ds_read_b128 v[32:35], v72 offset:1216\n\t" \
  "s_waitcnt lgkmcnt(2)\n\t" \
  "v_mfma_f32_32x32x16_bf16 v[0:15], v[40:43], v[244:247], v[0:15]\n\t" \
  "global_load_lds_dwordx4 v82, s[44:45] offset:2048\n\t" \
  "s_add_u32 s44, s44, 786432\n\t" \
  "s_addc_u32 s45, s45, 0\n\t" \
  "ds_read_b128 v[252:255], v71 offset:34816\n\t" \
  "ds_read_b128 v[40:43], v72 offset:1248\n\t" \
  "s_waitcnt lgkmcnt(2)\n\t" \
  "v_mfma_f32_32x32x16_bf16 v[0:15], v[32:35], v[248:251], v[0:15]\n\t" \
  "s_add_u32 m0, %[w3k], 139264\n\t" \
  "s_nop 0\n\t" \
  "global_load_lds_dwordx4 v82, s[46:47] offset:2048\n\t" \
  "s_waitcnt lgkmcnt(0)\n\t" \
  "s_waitcnt vmcnt(9)\n\t" \
  "s_barrier\n\t" \
  "ds_read_b64_tr_b16 v[32:33], v74 offset:0\n\t" \
  "ds_read_b64_tr_b16 v[34:35], v74 offset:768\n\t" \
  "ds_read_b64_tr_b16 v[36:37], v74 offset:64\n\t" \
  "ds_read_b64_tr_b16 v[38:39], v74 offset:832\n\t" \
  "v_mfma_f32_32x32x16_bf16 v[0:15], v[40:43], v[252:255], v[0:15]\n\t" \
  "s_bitcmp1_b32 %[fl], 1\n\t" \
  "s_cbranch_scc0 La1212_%=\n\t" \
  "v_add_u32 v89, 1, v89\n\t" \
  "global_store_dword v87, v89, %[fb] sc1\n\t" \
  "La1212_%=:\n\t" \
  "s_nop 7\n\t" \
  "s_nop 3\n\t" \
  "v_mul_f32 v52, 0xbfb8aa3b, v0\n\t" \
  "v_exp_f32 v52, v52\n\t" \
  "s_nop 0\n\t" \
  "v_add_f32 v52, 1.0, v52\n\t" \
  "v_rcp_f32 v52, v52\n\t" \
  "s_bitcmp0_b32 %[fl], 2\n\t" \
  "s_cselect_b32 exec_lo, -1, 0\n\t" \
  "s_mov_b32 exec_hi, 0\n\t" \
  "global_store_dword v77, v52, %[ws] sc1\n\t" \
  "s_mov_b64 exec, -1\n\t" \
  "s_branch Ea14_%=\n\t" \
  "Sa141_%=:\n\t" \
  "s_waitcnt lgkmcnt(5)\n\t" \
  "v_mfma_f32_32x32x16_bf16 v[0:15], v[148:151], v[40:43], v[0:15]\n\t" \
  "v_mfma_f32_32x32x16_bf16 v[16:31], v[148:151], v[44:47], v[16:31]\n\t" \
  "s_add_u32 m0, %[w2k], 16384\n\t" \
  "s_nop 0\n\t" \
  "global_load_lds_dwordx4 v66, s[42:43]\n\t" \
  "ds_read_b128 v[156:159], v71 offset:34816\n\t" \
  "ds_read_b64_tr_b16 v[40:41], v72 offset:47104\n\t" \
  "ds_read_b64_tr_b16 v[42:43], v72 offset:48128\n\t" \
  "ds_read_b64_tr_b16 v[44:45], v73 offset:47104\n\t" \
  "ds_read_b64_tr_b16 v[46:47], v73 offset:48128\n\t" \
  "s_waitcnt lgkmcnt(5)\n\t" \
  "v_mfma_f32_32x32x16_bf16 v[0:15], v[152:155], v[32:35], v[0:15]\n\t" \
  "v_mfma_f32_32x32x16_bf16 v[16:31], v[152:155], v[36:39], v[16:31]\n\t" \
  "global_load_lds_dwordx4 v67, s[42:43] offset:1024\n\t" \
  "s_add_u32 s42, s42, 245760\n\t" \
  "s_addc_u32 s43, s43, 0\n\t" \
  "s_add_u32 s46, s44, 1179648\n\t" \
  "s_addc_u32 s47, s45, 0\n\t" \
  "v_mov_b32 v91, 0\n\t" \
  "v_mov_b32 v92, 0\n\t" \
  "v_mov_b32 v93, 0\n\t" \
  "v_mov_b32 v95, 0\n\t" \
  "v_lshrrev_b32 v252, 5, v75\n\t" \
  "v_min_u32 v252, 9, v252\n\t" \
  "v_mul_u32_u24 v252, 3072, v252\n\t" \
  "v_and_b32 v253, 16, v75\n\t" \
  "v_lshl_add_u32 v253, v253, 3, s76\n\t" \
  "v_add_u32 v252, v252, v253\n\t" \
  "global_load_dword v252, v252, s[44:45]\n\t" \
  "s_waitcnt lgkmcnt(0)\n\t" \
  "s_waitcnt vmcnt(9)\n\t" \
  "s_barrier\n\t" \
  "v_add_u32 v68, 69632, v68\n\t" \
  "v_add_u32 v69, 69632, v69\n\t" \
  "v_add_u32 v70, 69632, v70\n\t" \
  "v_add_u32 v71, 69632, v71\n\t" \
  "v_add_u32 v72, 69632, v72\n\t" \
  "v_add_u32 v73, 69632, v73\n\t" \
  "ds_read_b128 v[160:163], v68 offset:0\n\t" \
  "ds_read_b64_tr_b16 v[32:33], v72 offset:0\n\t" \
  "ds_read_b64_tr_b16 v[34:35], v72 offset:1024\n\t" \
  "ds_read_b64_tr_b16 v[36:37], v73 offset:0\n\t" \
  "ds_read_b64_tr_b16 v[38:39], v73 offset:1024\n\t" \
  "v_mfma_f32_32x32x16_bf16 v[0:15], v[156:159], v[40:43], v[0:15]\n\t" \
  "v_mfma_f32_32x32x16_bf16 v[16:31], v[156:159], v[44:47], v[16:31]\n\t" \
  "s_bitcmp1_b32 %[fl], 1\n\t" \
  "s_cbranch_scc0 La116_%=\n\t" \
  "v_add_u32 v89, 1, v89\n\t" \
  "global_store_dword v87, v89, %[fb] sc1\n\t" \
  "La116_%=:\n\t" \
  "s_add_u32 m0, %[w2k], 34816\n\t" \
  "s_nop 0\n\t" \
  "global_load_lds_dwordx4 v64, s[40:41]\n\t" \
  "ds_read_b128 v[164:167], v69 offset:0\n\t" \
  "ds_read_b64_tr_b16 v[40:41], v72 offset:4096\n\t" \
  "ds_read_b64_tr_b16 v[42:43], v72 offset:5120\n\t" \
  "ds_read_b64_tr_b16 v[44:45], v73 offset:4096\n\t" \
  "ds_read_b64_tr_b16 v[46:47], v73 offset:5120\n\t" \
  "s_waitcnt lgkmcnt(5)\n\t" \
  "v_mfma_f32_32x32x16_bf16 v[0:15], v[160:163], v[32:35], v[0:15]\n\t" \
  "v_mfma_f32_32x32x16_bf16 v[16:31], v[160:163], v[36:39], v[16:31]\n\t" \
  "global_load_lds_dwordx4 v65, s[40:41] offset:1024\n\t" \
  "s_add_u32 s40, s40, 128\n\t" \
  "s_addc_u32 s41, s41, 0\n\t" \
  "global_load_ushort v90, v83, %[b1]\n\t" \
  "ds_read_b128 v[168:171], v70 offset:0\n\t" \
  "ds_read_b64_tr_b16 v[32:33], v72 offset:8192\n\t" \
  "ds_read_b64_tr_b16 v[34:35], v72 offset:9216\n\t" \
  "ds_read_b64_tr_b16 v[36:37], v73 offset:8192\n\t" \
  "ds_read_b64_tr_b16 v[38:39], v73 offset:9216\n\t" \
  "s_waitcnt lgkmcnt(5)\n\t" \
  "v_mfma_f32_32x32x16_bf16 v[0:15], v[164:167], v[40:43], v[0:15]\n\t" \
  "v_mfma_f32_32x32x16_bf16 v[16:31], v[164:167], v[44:47], v[16:31]\n\t" \
  "s_add_u32 m0, %[w2k], 51200\n\t" \
  "s_nop 0\n\t" \
  "global_load_lds_dwordx4 v66, s[42:43]\n\t" \
  "global_load_ushort v94, v83, %[b1] offset:64\n\t" \
  "ds_read_b128 v[172:175], v71 offset:0\n\t" \
  "ds_read_b64_tr_b16 v[40:41], v72 offset:12288\n\t" \
  "ds_read_b64_tr_b16 v[42:43], v72 offset:13312\n\t" \
  "ds_read_b64_tr_b16 v[44:45], v73 offset:12288\n\t" \
  "ds_read_b64_tr_b16 v[46:47], v73 offset:13312\n\t" \
  "s_waitcnt lgkmcnt(5)\n\t" \
  "v_mfma_f32_32x32x16_bf16 v[0:15], v[168:171], v[32:35], v[0:15]\n\t" \
  "v_mfma_f32_32x32x16_bf16 v[16:31], v[168:171], v[36:39], v[16:31]\n\t" \
  "global_load_lds_dwordx4 v67, s[42:43] offset:1024\n\t" \
  "s_add_u32 s42, s42, 245760\n\t" \
  "s_addc_u32 s43, s43, 0\n\t" \
  "s_waitcnt lgkmcnt(0)\n\t" \
  "s_waitcnt vmcnt(11)\n\t" \
  "s_barrier\n\t" \
  "ds_read_b128 v[176:179], v68 offset:34816\n\t" \
  "ds_read_b64_tr_b16 v[32:33], v72 offset:34816\n\t" \
  "ds_read_b64_tr_b16 v[34:35], v72 offset:35840\n\t" \
  "ds_read_b64_tr_b16 v[36:37], v73 offset:34816\n\t" \
  "ds_read_b64_tr_b16 v[38:39], v73 offset:35840\n\t" \
  "v_mfma_f32_32x32x16_bf16 v[0:15], v[172:175], v[40:43], v[0:15]\n\t" \
  "v_mfma_f32_32x32x16_bf16 v[16:31], v[172:175], v[44:47], v[16:31]\n\t" \
  "s_bitcmp1_b32 %[fl], 1\n\t" \
  "s_cbranch_scc0 La117_%=\n\t" \
  "v_add_u32 v89, 1, v89\n\t" \
  "global_store_dword v87, v89, %[fb] sc1\n\t" \
  "La117_%=:\n\t" \
  "s_add_u32 m0, %[w2k], 69632\n\t" \
  "s_nop 0\n\t" \
  "global_load_lds_dwordx4 v64, s[40:41]\n\t" \
  "ds_read_b128 v[180:183], v69 offset:34816\n\t" \
  "ds_read_b64_tr_b16 v[40:41], v72 offset:38912\n\t" \
  "ds_read_b64_tr_b16 v[42:43], v72 offset:39936\n\t" \
  "ds_read_b64_tr_b16 v[44:45], v73 offset:38912\n\t" \
  "ds_read_b64_tr_b16 v[46:47], v73 offset:39936\n\t" \
  "s_waitcnt lgkmcnt(5)\n\t" \
  "v_mfma_f32_32x32x16_bf16 v[0:15], v[176:179], v[32:35], v[0:15]\n\t" \
  "v_mfma_f32_32x32x16_bf16 v[16:31], v[176:179], v[36:39], v[16:31]\n\t" \
  "global_load_lds_dwordx4 v65, s[40:41] offset:1024\n\t" \
  "s_add_u32 s40, s40, 128\n\t" \
  "s_addc_u32 s41, s41, 0\n\t" \
  "ds_read_b128 v[184:187], v70 offset:34816\n\t" \
  "ds_read_b64_tr_b16 v[32:33], v72 offset:43008\n\t" \
  "ds_read_b64_tr_b16 v[34:35], v72 offset:44032\n\t" \
  "ds_read_b64_tr_b16 v[36:37], v73 offset:43008\n\t" \
  "ds_read_b64_tr_b16 v[38:39], v73 offset:44032\n\t" \
  "s_waitcnt lgkmcnt(5)\n\t" \
  "v_mfma_f32_32x32x16_bf16 v[0:15], v[180:183], v[40:43], v[0:15]\n\t" \
  "v_mfma_f32_32x32x16_bf16 v[16:31], v[180:183], v[44:47], v[16:31]\n\t" \
  "s_add_u32 m0, %[w2k], 86016\n\t" \
  "s_nop 0\n\t" \
  "global_load_lds_dwordx4 v66, s[42:43]\n\t" \
  "ds_read_b128 v[188:191], v71 offset:34816\n\t" \
  "ds_read_b64_tr_b16 v[40:41], v72 offset:47104\n\t" \
  "ds_read_b64_tr_b16 v[42:43], v72 offset:48128\n\t" \
  "ds_read_b64_tr_b16 v[44:45], v73 offset:47104\n\t" \
  "ds_read_b64_tr_b16 v[46:47], v73 offset:48128\n\t" \
  "s_waitcnt lgkmcnt(5)\n\t" \
  "v_mfma_f32_32x32x16_bf16 v[0:15], v[184:187], v[32:35], v[0:15]\n\t" \
  "v_mfma_f32_32x32x16_bf16 v[16:31], v[184:187], v[36:39], v[16:31]\n\t" \
  "global_load_lds_dwordx4 v67, s[42:43] offset:1024\n\t" \
  "s_add_u32 s42, s42, 245760\n\t" \
  "s_addc_u32 s43, s43, 0\n\t" \
  "s_waitcnt lgkmcnt(0)\n\t" \
  "s_waitcnt vmcnt(11)\n\t" \
  "s_barrier\n\t" \
  "v_add_u32 v68, -69632, v68\n\t" \
  "v_add_u32 v69, -69632, v69\n\t" \
  "v_add_u32 v70, -69632, v70\n\t" \
  "v_add_u32 v71, -69632, v71\n\t" \
  "v_add_u32 v72, -69632, v72\n\t" \
  "v_add_u32 v73, -69632, v73\n\t" \
  "ds_read_b128 v[192:195], v68 offset:0\n\t" \
  "ds_read_b64_tr_b16 v[32:33], v72 offset:0\n\t" \
  "ds_read_b64_tr_b16 v[34:35], v72 offset:1024\n\t" \
  "ds_read_b64_tr_b16 v[36:37], v73 offset:0\n\t" \
  "ds_read_b64_tr_b16 v[38:39], v73 offset:1024\n\t" \
  "v_mfma_f32_32x32x16_bf16 v[0:15], v[188:191], v[40:43], v[0:15]\n\t" \
  "v_mfma_f32_32x32x16_bf16 v[16:31], v[188:191], v[44:47], v[16:31]\n\t" \
  "s_bitcmp1_b32 %[fl], 1\n\t" \
  "s_cbranch_scc0 La118_%=\n\t" \
  "v_add_u32 v89, 1, v89\n\t" \
  "global_store_dword v87, v89, %[fb] sc1\n\t" \
  "La118_%=:\n\t" \
  "s_add_u32 m0, %[w2k], 104448\n\t" \
  "s_nop 0\n\t" \
  "global_load_lds_dwordx4 v64, s[40:41]\n\t" \
  "ds_read_b128 v[196:199], v69 offset:0\n\t" \
  "ds_read_b64_tr_b16 v[40:41], v72 offset:4096\n\t" \
  "ds_read_b64_tr_b16 v[42:43], v72 offset:5120\n\t" \
  "ds_read_b64_tr_b16 v[44:45], v73 offset:4096\n\t" \
  "ds_read_b64_tr_b16 v[46:47], v73 offset:5120\n\t" \
  "s_waitcnt lgkmcnt(5)\n\t" \
  "v_mfma_f32_32x32x16_bf16 v[0:15], v[192:195], v[32:35], v[0:15]\n\t" \
  "v_mfma_f32_32x32x16_bf16 v[16:31], v[192:195], v[36:39], v[16:31]\n\t" \
  "global_load_lds_dwordx4 v65, s[40:41] offset:1024\n\t" \
  "s_add_u32 s40, s40, 128\n\t" \
  "s_addc_u32 s41, s41, 0\n\t" \
  "ds_read_b128 v[200:203], v70 offset:0\n\t" \
  "ds_read_b64_tr_b16 v[32:33], v72 offset:8192\n\t" \
  "ds_read_b64_tr_b16 v[34:35], v72 offset:9216\n\t" \
  "ds_read_b64_tr_b16 v[36:37], v73 offset:8192\n\t" \
  "ds_read_b64_tr_b16 v[38:39], v73 offset:9216\n\t" \
  "s_waitcnt lgkmcnt(5)\n\t" \
  "v_mfma_f32_32x32x16_bf16 v[0:15], v[196:199], v[40:43], v[0:15]\n\t" \
  "v_mfma_f32_32x32x16_bf16 v[16:31], v[196:199], v[44:47], v[16:31]\n\t" \
  "s_add_u32 m0, %[w2k], 120832\n\t" \
  "s_nop 0\n\t" \
  "global_load_lds_dwordx4 v66, s[42:43]\n\t" \
  "ds_read_b128 v[204:207], v71 offset:0\n\t" \
  "ds_read_b64_tr_b16 v[40:41], v72 offset:12288\n\t" \
  "ds_read_b64_tr_b16 v[42:43], v72 offset:13312\n\t" \
  "ds_read_b64_tr_b16 v[44:45], v73 offset:12288\n\t" \
  "ds_read_b64_tr_b16 v[46:47], v73 offset:13312\n\t" \
  "s_waitcnt lgkmcnt(5)\n\t" \
  "v_mfma_f32_32x32x16_bf16 v[0:15], v[200:203], v[32:35], v[0:15]\n\t" \
  "v_mfma_f32_32x32x16_bf16 v[16:31], v[200:203], v[36:39], v[16:31]\n\t" \
  "global_load_lds_dwordx4 v67, s[42:43] offset:1024\n\t" \
  "s_add_u32 s42, s42, 245760\n\t" \
  "s_addc_u32 s43, s43, 0\n\t" \
  "s_waitcnt lgkmcnt(0)\n\t" \
  "s_waitcnt vmcnt(8)\n\t" \
  "s_barrier\n\t" \
  "ds_read_b128 v[208:211], v68 offset:34816\n\t" \
  "ds_read_b64_tr_b16 v[32:33], v72 offset:34816\n\t" \
  "ds_read_b64_tr_b16 v[34:35], v72 offset:35840\n\t" \
  "ds_read_b64_tr_b16 v[36:37], v73 offset:34816\n\t" \
  "ds_read_b64_tr_b16 v[38:39], v73 offset:35840\n\t" \
  "v_mfma_f32_32x32x16_bf16 v[0:15], v[204:207], v[40:43], v[0:15]\n\t" \
  "v_mfma_f32_32x32x16_bf16 v[16:31], v[204:207], v[44:47], v[16:31]\n\t" \
  "s_bitcmp1_b32 %[fl], 1\n\t" \
  "s_cbranch_scc0 La119_%=\n\t" \
  "v_add_u32 v89, 1, v89\n\t" \
  "global_store_dword v87, v89, %[fb] sc1\n\t" \
  "La119_%=:\n\t" \
  "s_add_u32 m0, %[w3k], 0\n\t" \
  "s_nop 0\n\t" \
  "global_load_lds_dwordx4 v80, s[44:45]\n\t" \
  "ds_read_b128 v[212:215], v69 offset:34816\n\t" \
  "ds_read_b64_tr_b16 v[40:41], v72 offset:38912\n\t" \
  "ds_read_b64_tr_b16 v[42:43], v72 offset:39936\n\t" \
  "ds_read_b64_tr_b16 v[44:45], v73 offset:38912\n\t" \
  "ds_read_b64_tr_b16 v[46:47], v73 offset:39936\n\t" \
  "s_waitcnt lgkmcnt(5)\n\t" \
  "v_mfma_f32_32x32x16_bf16 v[0:15], v[208:211], v[32:35], v[0:15]\n\t" \
  "v_mfma_f32_32x32x16_bf16 v[16:31], v[208:211], v[36:39], v[16:31]\n\t" \
  "global_load_lds_dwordx4 v81, s[44:45] offset:1024\n\t" \
  "ds_read_b128 v[216:219], v70 offset:34816\n\t" \
  "ds_read_b64_tr_b16 v[32:33], v72 offset:43008\n\t" \
  "ds_read_b64_tr_b16 v[34:35], v72 offset:44032\n\t" \
  "ds_read_b64_tr_b16 v[36:37], v73 offset:43008\n\t" \
  "ds_read_b64_tr_b16 v[38:39], v73 offset:44032\n\t" \
  "s_waitcnt lgkmcnt(5)\n\t" \
  "v_mfma_f32_32x32x16_bf16 v[0:15], v[212:215], v[40:43], v[0:15]\n\t" \
  "v_mfma_f32_32x32x16_bf16 v[16:31], v[212:215], v[44:47], v[16:31]\n\t" \
  "global_load_lds_dwordx4 v82, s[44:45] offset:2048\n\t" \
  "s_add_u32 s44, s44, 393216\n\t" \
  "s_addc_u32 s45, s45, 0\n\t" \
  "ds_read_b128 v[220:223], v71 offset:34816\n\t" \
  "ds_read_b64_tr_b16 v[40:41], v72 offset:47104\n\t" \
  "ds_read_b64_tr_b16 v[42:43], v72 offset:48128\n\t" \
  "ds_read_b64_tr_b16 v[44:45], v73 offset:47104\n\t" \
  "ds_read_b64_tr_b16 v[46:47], v73 offset:48128\n\t" \
  "s_waitcnt lgkmcnt(5)\n\t" \
  "v_mfma_f32_32x32x16_bf16 v[0:15], v[216:219], v[32:35], v[0:15]\n\t" \
  "v_mfma_f32_32x32x16_bf16 v[16:31], v[216:219], v[36:39], v[16:31]\n\t" \
  "s_add_u32 m0, %[w3k], 139264\n\t" \
  "s_nop 0\n\t" \
  "global_load_lds_dwordx4 v80, s[46:47]\n\t" \
  "v_lshrrev_b32 v64, 9, v75\n\t" \
  "v_mov_b32 v65, 0\n\t" \
  "v_mov_b32 v67, 0\n\t" \
  "v_mul_u32_u24 v64, 0x3f80, v64\n\t" \
  "v_mov_b32 v66, v64\n\t" \
  "s_waitcnt lgkmcnt(0)\n\t" \
  "s_waitcnt vmcnt(8)\n\t" \
  "s_barrier\n\t" \
  "v_add_u32 v68, 69632, v68\n\t" \
  "v_add_u32 v69, 69632, v69\n\t" \
  "v_add_u32 v70, 69632, v70\n\t" \
  "v_add_u32 v71, 69632, v71\n\t" \
  "v_add_u32 v72, 69632, v72\n\t" \
  "v_add_u32 v73, 69632, v73\n\t" \
  "ds_read_b128 v[224:227], v68 offset:0\n\t" \
  "ds_read_b64_tr_b16 v[32:33], v72 offset:0\n\t" \
  "ds_read_b64_tr_b16 v[34:35], v72 offset:1024\n\t" \
  "ds_read_b64_tr_b16 v[36:37], v73 offset:0\n\t" \
  "ds_read_b64_tr_b16 v[38:39], v73 offset:1024\n\t" \
  "v_mfma_f32_32x32x16_bf16 v[0:15], v[220:223], v[40:43], v[0:15]\n\t" \
  "v_mfma_f32_32x32x16_bf16 v[16:31], v[220:223], v[44:47], v[16:31]\n\t" \
  "s_bitcmp1_b32 %[fl], 1\n\t" \
  "s_cbranch_scc0 La1110_%=\n\t" \
  "v_add_u32 v89, 1, v89\n\t" \
  "global_store_dword v87, v89, %[fb] sc1\n\t" \
  "La1110_%=:\n\t" \
  "s_add_u32 m0, %[w3k], 34816\n\t" \
  "s_nop 0\n\t" \
  "global_load_lds_dwordx4 v80, s[44:45]\n\t" \
  "ds_read_b128 v[228:231], v69 offset:0\n\t" \
  "ds_read_b64_tr_b16 v[40:41], v72 offset:4096\n\t" \
  "ds_read_b64_tr_b16 v[42:43], v72 offset:5120\n\t" \
  "ds_read_b64_tr_b16 v[44:45], v73 offset:4096\n\t" \
  "ds_read_b64_tr_b16 v[46:47], v73 offset:5120\n\t" \
  "s_waitcnt lgkmcnt(5)\n\t" \
  "v_mfma_f32_32x32x16_bf16 v[0:15], v[224:227], v[32:35], v[0:15]\n\t" \
  "v_mfma_f32_32x32x16_bf16 v[16:31], v[224:227], v[36:39], v[16:31]\n\t" \
  "global_load_lds_dwordx4 v81, s[44:45] offset:1024\n\t" \
  "ds_read_b128 v[232:235], v70 offset:0\n\t" \
  "ds_read_b64_tr_b16 v[32:33], v72 offset:8192\n\t" \
  "ds_read_b64_tr_b16 v[34:35], v72 offset:9216\n\t" \
  "ds_read_b64_tr_b16 v[36:37], v73 offset:8192\n\t" \
  "ds_read_b64_tr_b16 v[38:39], v73 offset:9216\n\t" \
  "s_waitcnt lgkmcnt(5)\n\t" \
  "v_mfma_f32_32x32x16_bf16 v[0:15], v[228:231], v[40:43], v[0:15]\n\t" \
  "v_mfma_f32_32x32x16_bf16 v[16:31], v[228:231], v[44:47], v[16:31]\n\t" \
  "global_load_lds_dwordx4 v82, s[44:45] offset:2048\n\t" \
  "s_add_u32 s44, s44, 393216\n\t" \
  "s_addc_u32 s45, s45, 0\n\t" \
  "ds_read_b128 v[236:239], v71 offset:0\n\t" \
  "ds_read_b64_tr_b16 v[40:41], v72 offset:12288\n\t" \
  "ds_read_b64_tr_b16 v[42:43], v72 offset:13312\n\t" \
  "ds_read_b64_tr_b16 v[44:45], v73 offset:12288\n\t" \
  "ds_read_b64_tr_b16 v[46:47], v73 offset:13312\n\t" \
  "s_waitcnt lgkmcnt(5)\n\t" \
  "v_mfma_f32_32x32x16_bf16 v[0:15], v[232:235], v[32:35], v[0:15]\n\t" \
  "v_mfma_f32_32x32x16_bf16 v[16:31], v[232:235], v[36:39], v[16:31]\n\t" \
  "s_add_u32 m0, %[w3k], 139264\n\t" \
  "s_nop 0\n\t" \
  "global_load_lds_dwordx4 v81, s[46:47] offset:1024\n\t" \
  "s_waitcnt lgkmcnt(0)\n\t" \
  "s_waitcnt vmcnt(8)\n\t" \
  "s_barrier\n\t" \
  "ds_read_b128 v[240:243], v68 offset:34816\n\t" \
  "ds_read_b64_tr_b16 v[32:33], v72 offset:34816\n\t" \
  "ds_read_b64_tr_b16 v[34:35], v72 offset:35840\n\t" \
  "ds_read_b64_tr_b16 v[36:37], v73 offset:34816\n\t" \
  "ds_read_b64_tr_b16 v[38:39], v73 offset:35840\n\t" \
  "v_mfma_f32_32x32x16_bf16 v[0:15], v[236:239], v[40:43], v[0:15]\n\t" \
  "v_mfma_f32_32x32x16_bf16 v[16:31], v[236:239], v[44:47], v[16:31]\n\t" \
  "s_bitcmp1_b32 %[fl], 1\n\t" \
  "s_cbranch_scc0 La1111_%=\n\t" \
  "v_add_u32 v89, 1, v89\n\t" \
  "global_store_dword v87, v89, %[fb] sc1\n\t" \
  "La1111_%=:\n\t" \
  "s_add_u32 m0, %[w3k], 69632\n\t" \
  "s_nop 0\n\t" \
  "global_load_lds_dwordx4 v80, s[44:45]\n\t" \
  "ds_read_b128 v[244:247], v69 offset:34816\n\t" \
  "ds_read_b64_tr_b16 v[40:41], v72 offset:38912\n\t" \
  "ds_read_b64_tr_b16 v[42:43], v72 offset:39936\n\t" \
  "ds_read_b64_tr_b16 v[44:45], v73 offset:38912\n\t" \
  "ds_read_b64_tr_b16 v[46:47], v73 offset:39936\n\t" \
  "s_waitcnt lgkmcnt(5)\n\t" \
  "v_mfma_f32_32x32x16_bf16 v[0:15], v[240:243], v[32:35], v[0:15]\n\t" \
  "v_mfma_f32_32x32x16_bf16 v[16:31], v[240:243], v[36:39], v[16:31]\n\t" \
  "global_load_lds_dwordx4 v81, s[44:45] offset:1024\n\t" \
  "ds_read_b128 v[248:251], v70 offset:34816\n\t" \
  "ds_read_b64_tr_b16 v[32:33], v72 offset:43008\n\t" \
  "ds_read_b64_tr_b16 v[34:35], v72 offset:44032\n\t" \
  "ds_read_b64_tr_b16 v[36:37], v73 offset:43008\n\t" \
  "ds_read_b64_tr_b16 v[38:39], v73 offset:44032\n\t" \
  "s_waitcnt lgkmcnt(5)\n\t" \
  "v_mfma_f32_32x32x16_bf16 v[0:15], v[244:247], v[40:43], v[0:15]\n\t" \
  "v_mfma_f32_32x32x16_bf16 v[16:31], v[244:247], v[44:47], v[16:31]\n\t" \
  "global_load_lds_dwordx4 v82, s[44:45] offset:2048\n\t" \
  "s_add_u32 s44, s44, 786432\n\t" \
  "s_addc_u32 s45, s45, 0\n\t" \
  "ds_read_b128 v[252:255], v71 offset:34816\n\t" \
  "ds_read_b64_tr_b16 v[40:41], v72 offset:47104\n\t" \
  "ds_read_b64_tr_b16 v[42:43], v72 offset:48128\n\t" \
  "ds_read_b64_tr_b16 v[44:45], v73 offset:47104\n\t" \
  "ds_read_b64_tr_b16 v[46:47], v73 offset:48128\n\t" \
  "s_waitcnt lgkmcnt(5)\n\t" \
  "v_mfma_f32_32x32x16_bf16 v[0:15], v[248:251], v[32:35], v[0:15]\n\t" \
  "v_mfma_f32_32x32x16_bf16 v[16:31], v[248:251], v[36:39], v[16:31]\n\t" \
  "s_add_u32 m0, %[w3k], 139264\n\t" \
  "s_nop 0\n\t" \
  "global_load_lds_dwordx4 v82, s[46:47] offset:2048\n\t" \
  "s_waitcnt lgkmcnt(0)\n\t" \
  "s_waitcnt vmcnt(9)\n\t" \
  "s_barrier\n\t" \
  "ds_read_b64_tr_b16 v[32:33], v74 offset:0\n\t" \
  "ds_read_b64_tr_b16 v[34:35], v74 offset:768\n\t" \
  "ds_read_b64_tr_b16 v[36:37], v74 offset:64\n\t" \
  "ds_read_b64_tr_b16 v[38:39], v74 offset:832\n\t" \
  "v_mfma_f32_32x32x16_bf16 v[0:15], v[252:255], v[40:43], v[0:15]\n\t" \
  "v_mfma_f32_32x32x16_bf16 v[16:31], v[252:255], v[44:47], v[16:31]\n\t" \
  "s_bitcmp1_b32 %[fl], 1\n\t" \
  "s_cbranch_scc0 La1112_%=\n\t" \
  "v_add_u32 v89, 1, v89\n\t" \
  "global_store_dword v87, v89, %[fb] sc1\n\t" \
  "La1112_%=:\n\t" \
  "s_nop 7\n\t" \
  "s_nop 3\n\t" \
  "v_cvt_pk_bf16_f32 v48, v0, v1\n\t" \
  "v_cvt_pk_bf16_f32 v49, v2, v3\n\t" \
  "v_cvt_pk_bf16_f32 v50, v4, v5\n\t" \
  "v_cvt_pk_bf16_f32 v51, v6, v7\n\t" \
  "v_cvt_pk_bf16_f32 v52, v8, v9\n\t" \
  "v_cvt_pk_bf16_f32 v53, v10, v11\n\t" \
  "v_cvt_pk_bf16_f32 v54, v12, v13\n\t" \
  "v_cvt_pk_bf16_f32 v55, v14, v15\n\t" \
  "v_cvt_pk_bf16_f32 v56, v16, v17\n\t" \
  "v_cvt_pk_bf16_f32 v57, v18, v19\n\t" \
  "v_cvt_pk_bf16_f32 v58, v20, v21\n\t" \
  "v_cvt_pk_bf16_f32 v59, v22, v23\n\t" \
  "v_cvt_pk_bf16_f32 v60, v24, v25\n\t" \
  "v_cvt_pk_bf16_f32 v61, v26, v27\n\t" \
  "v_cvt_pk_bf16_f32 v62, v28, v29\n\t" \
  "v_cvt_pk_bf16_f32 v63, v30, v31\n\t" \
  "Ea14_%=:\n\t"
#define A2 \
  "s_nop 4\n\t" \
  "s_bitcmp1_b32 %[fl], 2\n\t" \
  "s_cbranch_scc1 Sa20_%=\n\t" \
  "v_mfma_f32_32x32x16_bf16 v[0:15], v[90:93], v[64:67], 0\n\t" \
  "v_mfma_f32_32x32x16_bf16 v[16:31], v[92:95], v[64:67], 0\n\t" \
  "ds_read_b64_tr_b16 v[40:41], v74 offset:3072\n\t" \
  "ds_read_b64_tr_b16 v[42:43], v74 offset:3840\n\t" \
  "ds_read_b64_tr_b16 v[44:45], v74 offset:3136\n\t" \
  "ds_read_b64_tr_b16 v[46:47], v74 offset:3904\n\t" \
  "s_waitcnt lgkmcnt(4)\n\t" \
  "v_mfma_f32_32x32x16_bf16 v[0:15], v[32:35], v[96:99], v[0:15]\n\t" \
  "v_mfma_f32_32x32x16_bf16 v[16:31], v[36:39], v[96:99], v[16:31]\n\t" \
  "s_bitcmp0_b32 %[fl], 8\n\t" \
  "s_cselect_b32 exec_lo, -1, 0\n\t" \
  "s_bitcmp0_b32 %[fl], 9\n\t" \
  "s_cselect_b32 exec_hi, -1, 0\n\t" \
  "global_store_dwordx4 v76, v[48:51], %[ws] sc1\n\t" \
  "s_bitcmp1_b32 %[fl], 7\n\t" \
  "s_cbranch_scc0 La201_%=\n\t" \
  "s_not_b64 exec, exec\n\t" \
  "s_cbranch_scc0 La201_%=\n\t" \
  "v_mad_i32_i24 v73, v83, -4, v76\n\t" \
  "global_store_dwordx2 v73, v[48:49], %[ws] sc1\n\t" \
  "global_store_dwordx2 v73, v[50:51], s[64:65] sc1\n\t" \
  "La201_%=:\n\t" \
  "s_mov_b64 exec, -1\n\t" \
  "ds_read_b64_tr_b16 v[32:33], v74 offset:6144\n\t" \
  "ds_read_b64_tr_b16 v[34:35], v74 offset:6912\n\t" \
  "ds_read_b64_tr_b16 v[36:37], v74 offset:6208\n\t" \
  "ds_read_b64_tr_b16 v[38:39], v74 offset:6976\n\t" \
  "s_waitcnt lgkmcnt(4)\n\t" \
  "v_mfma_f32_32x32x16_bf16 v[0:15], v[40:43], v[100:103], v[0:15]\n\t" \
  "v_mfma_f32_32x32x16_bf16 v[16:31], v[44:47], v[100:103], v[16:31]\n\t" \
  "ds_read_b64_tr_b16 v[40:41], v74 offset:9216\n\t" \
  "ds_read_b64_tr_b16 v[42:43], v74 offset:9984\n\t" \
  "ds_read_b64_tr_b16 v[44:45], v74 offset:9280\n\t" \
  "ds_read_b64_tr_b16 v[46:47], v74 offset:10048\n\t" \
  "s_waitcnt lgkmcnt(4)\n\t" \
  "v_mfma_f32_32x32x16_bf16 v[0:15], v[32:35], v[104:107], v[0:15]\n\t" \
  "v_mfma_f32_32x32x16_bf16 v[16:31], v[36:39], v[104:107], v[16:31]\n\t" \
  "s_bitcmp0_b32 %[fl], 10\n\t" \
  "s_cselect_b32 exec_lo, -1, 0\n\t" \
  "s_bitcmp0_b32 %[fl], 11\n\t" \
  "s_cselect_b32 exec_hi, -1, 0\n\t" \
  "global_store_dwordx4 v77, v[52:55], %[ws] sc1\n\t" \
  "s_bitcmp1_b32 %[fl], 7\n\t" \
  "s_cbranch_scc0 La202_%=\n\t" \
  "s_not_b64 exec, exec\n\t" \
  "s_cbranch_scc0 La202_%=\n\t" \
  "v_mad_i32_i24 v73, v83, -4, v77\n\t" \
  "global_store_dwordx2 v73, v[52:53], %[ws] sc1\n\t" \
  "global_store_dwordx2 v73, v[54:55], s[64:65] sc1\n\t" \
  "La202_%=:\n\t" \
  "s_mov_b64 exec, -1\n\t" \
  "ds_read_b64_tr_b16 v[32:33], v74 offset:12288\n\t" \
  "ds_read_b64_tr_b16 v[34:35], v74 offset:13056\n\t" \
  "ds_read_b64_tr_b16 v[36:37], v74 offset:12352\n\t" \
  "ds_read_b64_tr_b16 v[38:39], v74 offset:13120\n\t" \
  "s_waitcnt lgkmcnt(4)\n\t" \
  "v_mfma_f32_32x32x16_bf16 v[0:15], v[40:43], v[108:111], v[0:15]\n\t" \
  "v_mfma_f32_32x32x16_bf16 v[16:31], v[44:47], v[108:111], v[16:31]\n\t" \
  "ds_read_b64_tr_b16 v[40:41], v74 offset:15360\n\t" \
  "ds_read_b64_tr_b16 v[42:43], v74 offset:16128\n\t" \
  "ds_read_b64_tr_b16 v[44:45], v74 offset:15424\n\t" \
  "ds_read_b64_tr_b16 v[46:47], v74 offset:16192\n\t" \
  "s_waitcnt lgkmcnt(4)\n\t" \
  "v_mfma_f32_32x32x16_bf16 v[0:15], v[32:35], v[112:115], v[0:15]\n\t" \
  "v_mfma_f32_32x32x16_bf16 v[16:31], v[36:39], v[112:115], v[16:31]\n\t" \
  "s_bitcmp0_b32 %[fl], 12\n\t" \
  "s_cselect_b32 exec_lo, -1, 0\n\t" \
  "s_bitcmp0_b32 %[fl], 13\n\t" \
  "s_cselect_b32 exec_hi, -1, 0\n\t" \
  "global_store_dwordx4 v78, v[56:59], %[ws] sc1\n\t" \
  "s_bitcmp1_b32 %[fl], 7\n\t" \
  "s_cbranch_scc0 La203_%=\n\t" \
  "s_not_b64 exec, exec\n\t" \
  "s_cbranch_scc0 La203_%=\n\t" \
  "v_mad_i32_i24 v73, v83, -4, v78\n\t" \
  "global_store_dwordx2 v73, v[56:57], %[ws] sc1\n\t" \
  "global_store_dwordx2 v73, v[58:59], s[64:65] sc1\n\t" \
  "La203_%=:\n\t" \
  "s_mov_b64 exec, -1\n\t" \
  "ds_read_b64_tr_b16 v[32:33], v74 offset:18432\n\t" \
  "ds_read_b64_tr_b16 v[34:35], v74 offset:19200\n\t" \
  "ds_read_b64_tr_b16 v[36:37], v74 offset:18496\n\t" \
  "ds_read_b64_tr_b16 v[38:39], v74 offset:19264\n\t" \
  "s_waitcnt lgkmcnt(4)\n\t" \
  "v_mfma_f32_32x32x16_bf16 v[0:15], v[40:43], v[116:119], v[0:15]\n\t" \
  "v_mfma_f32_32x32x16_bf16 v[16:31], v[44:47], v[116:119], v[16:31]\n\t" \
  "ds_read_b64_tr_b16 v[40:41], v74 offset:21504\n\t" \
  "ds_read_b64_tr_b16 v[42:43], v74 offset:22272\n\t" \
  "ds_read_b64_tr_b16 v[44:45], v74 offset:21568\n\t" \
  "ds_read_b64_tr_b16 v[46:47], v74 offset:22336\n\t" \
  "s_waitcnt lgkmcnt(4)\n\t" \
  "v_mfma_f32_32x32x16_bf16 v[0:15], v[32:35], v[120:123], v[0:15]\n\t" \
  "v_mfma_f32_32x32x16_bf16 v[16:31], v[36:39], v[120:123], v[16:31]\n\t" \
  "s_bitcmp0_b32 %[fl], 14\n\t" \
  "s_cselect_b32 exec_lo, -1, 0\n\t" \
  "s_bitcmp0_b32 %[fl], 15\n\t" \
  "s_cselect_b32 exec_hi, -1, 0\n\t" \
  "global_store_dwordx4 v79, v[60:63], %[ws] sc1\n\t" \
  "s_bitcmp1_b32 %[fl], 7\n\t" \
  "s_cbranch_scc0 La204_%=\n\t" \
  "s_not_b64 exec, exec\n\t" \
  "s_cbranch_scc0 La204_%=\n\t" \
  "v_mad_i32_i24 v73, v83, -4, v79\n\t" \
  "global_store_dwordx2 v73, v[60:61], %[ws] sc1\n\t" \
  "global_store_dwordx2 v73, v[62:63], s[64:65] sc1\n\t" \
  "La204_%=:\n\t" \
  "s_mov_b64 exec, -1\n\t" \
  "s_waitcnt vmcnt(4)\n\t" \
  "s_waitcnt lgkmcnt(0)\n\t" \
  "s_barrier\n\t" \
  "ds_read_b64_tr_b16 v[32:33], v74 offset:34816\n\t" \
  "ds_read_b64_tr_b16 v[34:35], v74 offset:35584\n\t" \
  "ds_read_b64_tr_b16 v[36:37], v74 offset:34880\n\t" \
  "ds_read_b64_tr_b16 v[38:39], v74 offset:35648\n\t" \
  "v_mfma_f32_32x32x16_bf16 v[0:15], v[40:43], v[124:127], v[0:15]\n\t" \
  "v_mfma_f32_32x32x16_bf16 v[16:31], v[44:47], v[124:127], v[16:31]\n\t" \
  "ds_read_b64_tr_b16 v[40:41], v74 offset:37888\n\t" \
  "ds_read_b64_tr_b16 v[42:43], v74 offset:38656\n\t" \
  "ds_read_b64_tr_b16 v[44:45], v74 offset:37952\n\t" \
  "ds_read_b64_tr_b16 v[46:47], v74 offset:38720\n\t" \
  "s_waitcnt lgkmcnt(4)\n\t" \
  "v_mfma_f32_32x32x16_bf16 v[0:15], v[32:35], v[128:131], v[0:15]\n\t" \
  "v_mfma_f32_32x32x16_bf16 v[16:31], v[36:39], v[128:131], v[16:31]\n\t" \
  "s_add_u32 m0, %[w3k], 104448\n\t" \
  "s_nop 0\n\t" \
  "global_load_lds_dwordx4 v80, s[44:45]\n\t" \
  "ds_read_b64_tr_b16 v[32:33], v74 offset:40960\n\t" \
  "ds_read_b64_tr_b16 v[34:35], v74 offset:41728\n\t" \
  "ds_read_b64_tr_b16 v[36:37], v74 offset:41024\n\t" \
  "ds_read_b64_tr_b16 v[38:39], v74 offset:41792\n\t" \
  "s_waitcnt lgkmcnt(4)\n\t" \
  "v_mfma_f32_32x32x16_bf16 v[0:15], v[40:43], v[132:135], v[0:15]\n\t" \
  "v_mfma_f32_32x32x16_bf16 v[16:31], v[44:47], v[132:135], v[16:31]\n\t" \
  "v_mov_b32 v120, 0xdddddddd\n\t" \
  "v_add_u32 v121, 0, v75\n\t" \
  "v_bfe_u32 v127, v75, 4, 5\n\t" \
  "v_lshlrev_b32 v122, 3, v127\n\t" \
  "v_add_u32 v122, 1024, v122\n\t" \
  "global_load_lds_dwordx4 v81, s[44:45] offset:1024\n\t" \
  "ds_read_b64_tr_b16 v[40:41], v74 offset:44032\n\t" \
  "ds_read_b64_tr_b16 v[42:43], v74 offset:44800\n\t" \
  "ds_read_b64_tr_b16 v[44:45], v74 offset:44096\n\t" \
  "ds_read_b64_tr_b16 v[46:47], v74 offset:44864\n\t" \
  "s_waitcnt lgkmcnt(4)\n\t" \
  "v_mfma_f32_32x32x16_bf16 v[0:15], v[32:35], v[136:139], v[0:15]\n\t" \
  "v_mfma_f32_32x32x16_bf16 v[16:31], v[36:39], v[136:139], v[16:31]\n\t" \
  "v_lshrrev_b32 v123, 9, v75\n\t" \
  "v_mul_u32_u24 v123, 192, v123\n\t" \
  "v_sub_u32 v123, v75, v123\n\t" \
  "v_cmp_lt_u32 vcc, 27, v127\n\t" \
  "v_subrev_u32 v126, 192, v123\n\t" \
  "v_subrev_u32 v125, 20, v127\n\t" \
  "global_load_lds_dwordx4 v82, s[44:45] offset:2048\n\t" \
  "v_lshrrev_b32 v72, 6, v75\n\t" \
  "v_mul_u32_u24 v72, 1280, v72\n\t" \
  "v_and_or_b32 v72, v75, 48, v72\n\t" \
  "s_add_u32 m0, %[w2k], 0\n\t" \
  "s_nop 0\n\t" \
  "global_load_lds_dwordx4 v72, s[60:61]\n\t" \
  "s_add_u32 m0, %[w2k], 1024\n\t" \
  "s_nop 0\n\t" \
  "global_load_lds_dwordx4 v72, s[62:63]\n\t" \
  "ds_read_b64_tr_b16 v[32:33], v74 offset:47104\n\t" \
  "ds_read_b64_tr_b16 v[34:35], v74 offset:47872\n\t" \
  "ds_read_b64_tr_b16 v[36:37], v74 offset:47168\n\t" \
  "ds_read_b64_tr_b16 v[38:39], v74 offset:47936\n\t" \
  "s_waitcnt lgkmcnt(4)\n\t" \
  "v_mfma_f32_32x32x16_bf16 v[0:15], v[40:43], v[140:143], v[0:15]\n\t" \
  "v_mfma_f32_32x32x16_bf16 v[16:31], v[44:47], v[140:143], v[16:31]\n\t" \
  "ds_read_b64_tr_b16 v[40:41], v74 offset:50176\n\t" \
  "ds_read_b64_tr_b16 v[42:43], v74 offset:50944\n\t" \
  "ds_read_b64_tr_b16 v[44:45], v74 offset:50240\n\t" \
  "ds_read_b64_tr_b16 v[46:47], v74 offset:51008\n\t" \
  "s_waitcnt lgkmcnt(4)\n\t" \
  "v_mfma_f32_32x32x16_bf16 v[0:15], v[32:35], v[144:147], v[0:15]\n\t" \
  "v_mfma_f32_32x32x16_bf16 v[16:31], v[36:39], v[144:147], v[16:31]\n\t" \
  "v_cndmask_b32 v123, v123, v126, vcc\n\t" \
  "v_cmp_gt_u32 vcc, 8, v125\n\t" \
  "v_min_u32 v125, 1, v125\n\t" \
  "v_lshlrev_b32 v125, 4, v125\n\t" \
  "v_add_u32 v125, 640, v125\n\t" \
  "ds_read_b64_tr_b16 v[32:33], v74 offset:53248\n\t" \
  "ds_read_b64_tr_b16 v[34:35], v74 offset:54016\n\t" \
  "ds_read_b64_tr_b16 v[36:37], v74 offset:53312\n\t" \
  "ds_read_b64_tr_b16 v[38:39], v74 offset:54080\n\t" \
  "s_waitcnt lgkmcnt(4)\n\t" \
  "v_mfma_f32_32x32x16_bf16 v[0:15], v[40:43], v[148:151], v[0:15]\n\t" \
  "v_mfma_f32_32x32x16_bf16 v[16:31], v[44:47], v[148:151], v[16:31]\n\t" \
  "v_cndmask_b32 v123, v123, v125, vcc\n\t" \
  "v_add_u32 v123, 1280, v123\n\t" \
  "v_add_u32 v124, %[w3k], v75\n\t" \
  "v_add_u32 v124, 103424, v124\n\t" \
  "v_add_u32 v125, 12288, v124\n\t" \
  "ds_read_b64_tr_b16 v[40:41], v74 offset:56320\n\t" \
  "ds_read_b64_tr_b16 v[42:43], v74 offset:57088\n\t" \
  "ds_read_b64_tr_b16 v[44:45], v74 offset:56384\n\t" \
  "ds_read_b64_tr_b16 v[46:47], v74 offset:57152\n\t" \
  "s_waitcnt lgkmcnt(4)\n\t" \
  "v_mfma_f32_32x32x16_bf16 v[0:15], v[32:35], v[152:155], v[0:15]\n\t" \
  "v_mfma_f32_32x32x16_bf16 v[16:31], v[36:39], v[152:155], v[16:31]\n\t" \
  "v_add_u32 v126, s73, v75\n\t" \
  "s_mov_b64 s[40:41], %[ws]\n\t" \
  "s_add_u32 s44, s40, s83\n\t" \
  "s_addc_u32 s45, s41, 0\n\t" \
  "s_mul_i32 s50, %[w2k], 5\n\t" \
  "s_add_u32 s50, s50, 21504\n\t" \
  "v_add_u32 v74, 69632, v74\n\t" \
  "ds_read_b64_tr_b16 v[32:33], v74 offset:0\n\t" \
  "ds_read_b64_tr_b16 v[34:35], v74 offset:768\n\t" \
  "ds_read_b64_tr_b16 v[36:37], v74 offset:64\n\t" \
  "ds_read_b64_tr_b16 v[38:39], v74 offset:832\n\t" \
  "s_waitcnt lgkmcnt(4)\n\t" \
  "v_mfma_f32_32x32x16_bf16 v[0:15], v[40:43], v[156:159], v[0:15]\n\t" \
  "v_mfma_f32_32x32x16_bf16 v[16:31], v[44:47], v[156:159], v[16:31]\n\t" \
  "s_waitcnt vmcnt(5)\n\t" \
  "s_barrier\n\t" \
  "ds_read_b64_tr_b16 v[40:41], v74 offset:3072\n\t" \
  "ds_read_b64_tr_b16 v[42:43], v74 offset:3840\n\t" \
  "ds_read_b64_tr_b16 v[44:45], v74 offset:3136\n\t" \
  "ds_read_b64_tr_b16 v[46:47], v74 offset:3904\n\t" \
  "s_waitcnt lgkmcnt(4)\n\t" \
  "v_mfma_f32_32x32x16_bf16 v[0:15], v[32:35], v[160:163], v[0:15]\n\t" \
  "v_mfma_f32_32x32x16_bf16 v[16:31], v[36:39], v[160:163], v[16:31]\n\t" \
  "ds_read_b64_tr_b16 v[32:33], v74 offset:6144\n\t" \
  "ds_read_b64_tr_b16 v[34:35], v74 offset:6912\n\t" \
  "ds_read_b64_tr_b16 v[36:37], v74 offset:6208\n\t" \
  "ds_read_b64_tr_b16 v[38:39], v74 offset:6976\n\t" \
  "s_waitcnt lgkmcnt(4)\n\t" \
  "v_mfma_f32_32x32x16_bf16 v[0:15], v[40:43], v[164:167], v[0:15]\n\t" \
  "v_mfma_f32_32x32x16_bf16 v[16:31], v[44:47], v[164:167], v[16:31]\n\t" \
  "ds_read_b64_tr_b16 v[40:41], v74 offset:9216\n\t" \
  "ds_read_b64_tr_b16 v[42:43], v74 offset:9984\n\t" \
  "ds_read_b64_tr_b16 v[44:45], v74 offset:9280\n\t" \
  "ds_read_b64_tr_b16 v[46:47], v74 offset:10048\n\t" \
  "s_waitcnt lgkmcnt(4)\n\t" \
  "v_mfma_f32_32x32x16_bf16 v[0:15], v[32:35], v[168:171], v[0:15]\n\t" \
  "v_mfma_f32_32x32x16_bf16 v[16:31], v[36:39], v[168:171], v[16:31]\n\t" \
  "ds_read_b64_tr_b16 v[32:33], v74 offset:12288\n\t" \
  "ds_read_b64_tr_b16 v[34:35], v74 offset:13056\n\t" \
  "ds_read_b64_tr_b16 v[36:37], v74 offset:12352\n\t" \
  "ds_read_b64_tr_b16 v[38:39], v74 offset:13120\n\t" \
  "s_waitcnt lgkmcnt(4)\n\t" \
  "v_mfma_f32_32x32x16_bf16 v[0:15], v[40:43], v[172:175], v[0:15]\n\t" \
  "v_mfma_f32_32x32x16_bf16 v[16:31], v[44:47], v[172:175], v[16:31]\n\t" \
  "ds_read_b64_tr_b16 v[40:41], v74 offset:15360\n\t" \
  "ds_read_b64_tr_b16 v[42:43], v74 offset:16128\n\t" \
  "ds_read_b64_tr_b16 v[44:45], v74 offset:15424\n\t" \
  "ds_read_b64_tr_b16 v[46:47], v74 offset:16192\n\t" \
  "s_waitcnt lgkmcnt(4)\n\t" \
  "v_mfma_f32_32x32x16_bf16 v[0:15], v[32:35], v[176:179], v[0:15]\n\t" \
  "v_mfma_f32_32x32x16_bf16 v[16:31], v[36:39], v[176:179], v[16:31]\n\t" \
  "ds_read_b64_tr_b16 v[32:33], v74 offset:18432\n\t" \
  "ds_read_b64_tr_b16 v[34:35], v74 offset:19200\n\t" \
  "ds_read_b64_tr_b16 v[36:37], v74 offset:18496\n\t" \
  "ds_read_b64_tr_b16 v[38:39], v74 offset:19264\n\t" \
  "s_waitcnt lgkmcnt(4)\n\t" \
  "v_mfma_f32_32x32x16_bf16 v[0:15], v[40:43], v[180:183], v[0:15]\n\t" \
  "v_mfma_f32_32x32x16_bf16 v[16:31], v[44:47], v[180:183], v[16:31]\n\t" \
  "ds_read_b64_tr_b16 v[40:41], v74 offset:21504\n\t" \
  "ds_read_b64_tr_b16 v[42:43], v74 offset:22272\n\t" \
  "ds_read_b64_tr_b16 v[44:45], v74 offset:21568\n\t" \
  "ds_read_b64_tr_b16 v[46:47], v74 offset:22336\n\t" \
  "s_waitcnt lgkmcnt(4)\n\t" \
  "v_mfma_f32_32x32x16_bf16 v[0:15], v[32:35], v[184:187], v[0:15]\n\t" \
  "v_mfma_f32_32x32x16_bf16 v[16:31], v[36:39], v[184:187], v[16:31]\n\t" \
  "v_add_u32 v74, 69632, v74\n\t" \
  "ds_read_b64_tr_b16 v[32:33], v74 offset:0\n\t" \
  "ds_read_b64_tr_b16 v[34:35], v74 offset:768\n\t" \
  "ds_read_b64_tr_b16 v[36:37], v74 offset:64\n\t" \
  "ds_read_b64_tr_b16 v[38:39], v74 offset:832\n\t" \
  "s_waitcnt lgkmcnt(4)\n\t" \
  "v_mfma_f32_32x32x16_bf16 v[0:15], v[40:43], v[188:191], v[0:15]\n\t" \
  "v_mfma_f32_32x32x16_bf16 v[16:31], v[44:47], v[188:191], v[16:31]\n\t" \
  "ds_read_b64_tr_b16 v[40:41], v74 offset:3072\n\t" \
  "ds_read_b64_tr_b16 v[42:43], v74 offset:3840\n\t" \
  "ds_read_b64_tr_b16 v[44:45], v74 offset:3136\n\t" \
  "ds_read_b64_tr_b16 v[46:47], v74 offset:3904\n\t" \
  "s_waitcnt lgkmcnt(4)\n\t" \
  "v_mfma_f32_32x32x16_bf16 v[0:15], v[32:35], v[192:195], v[0:15]\n\t" \
  "v_mfma_f32_32x32x16_bf16 v[16:31], v[36:39], v[192:195], v[16:31]\n\t" \
  "ds_read_b64_tr_b16 v[32:33], v74 offset:6144\n\t" \
  "ds_read_b64_tr_b16 v[34:35], v74 offset:6912\n\t" \
  "ds_read_b64_tr_b16 v[36:37], v74 offset:6208\n\t" \
  "ds_read_b64_tr_b16 v[38:39], v74 offset:6976\n\t" \
  "s_waitcnt lgkmcnt(4)\n\t" \
  "v_mfma_f32_32x32x16_bf16 v[0:15], v[40:43], v[196:199], v[0:15]\n\t" \
  "v_mfma_f32_32x32x16_bf16 v[16:31], v[44:47], v[196:199], v[16:31]\n\t" \
  "ds_read_b64_tr_b16 v[40:41], v74 offset:9216\n\t" \
  "ds_read_b64_tr_b16 v[42:43], v74 offset:9984\n\t" \
  "ds_read_b64_tr_b16 v[44:45], v74 offset:9280\n\t" \
  "ds_read_b64_tr_b16 v[46:47], v74 offset:10048\n\t" \
  "s_waitcnt lgkmcnt(4)\n\t" \
  "v_mfma_f32_32x32x16_bf16 v[0:15], v[32:35], v[200:203], v[0:15]\n\t" \
  "v_mfma_f32_32x32x16_bf16 v[16:31], v[36:39], v[200:203], v[16:31]\n\t" \
  "ds_read_b64_tr_b16 v[32:33], v74 offset:12288\n\t" \
  "ds_read_b64_tr_b16 v[34:35], v74 offset:13056\n\t" \
  "ds_read_b64_tr_b16 v[36:37], v74 offset:12352\n\t" \
  "ds_read_b64_tr_b16 v[38:39], v74 offset:13120\n\t" \
  "v_mov_b64 v[184:185], 0\n\t" \
  "v_mov_b64 v[186:187], 0\n\t" \
  "v_mov_b64 v[188:189], 0\n\t" \
  "v_mov_b64 v[190:191], 0\n\t" \
  "v_mov_b64 v[192:193], 0\n\t" \
  "v_mov_b64 v[194:195], 0\n\t" \
  "v_mov_b64 v[196:197], 0\n\t" \
  "v_mov_b64 v[198:199], 0\n\t" \
  "s_bitcmp1_b32 %[fl], 17\n\t" \
  "s_cselect_b32 %[cnt], -1, 0\n\t" \
  "global_load_ushort v184, v83, s[68:69]\n\t" \
  "global_load_ushort v188, v83, s[68:69] offset:64\n\t" \
  "global_load_ushort v192, v83, s[70:71]\n\t" \
  "v_and_b32 v196, %[cnt], v64\n\t" \
  "v_and_b32 v71, 0x200, v75\n\t" \
  "v_add_u32 v70, s74, v75\n\t" \
  "v_lshl_add_u32 v71, v71, 2, v75\n\t" \
  "v_add_u32 v71, s75, v71\n\t" \
  "s_waitcnt lgkmcnt(4)\n\t" \
  "v_mfma_f32_32x32x16_bf16 v[0:15], v[40:43], v[204:207], v[0:15]\n\t" \
  "v_mfma_f32_32x32x16_bf16 v[16:31], v[44:47], v[204:207], v[16:31]\n\t" \
  "ds_read_b64_tr_b16 v[40:41], v74 offset:15360\n\t" \
  "ds_read_b64_tr_b16 v[42:43], v74 offset:16128\n\t" \
  "ds_read_b64_tr_b16 v[44:45], v74 offset:15424\n\t" \
  "ds_read_b64_tr_b16 v[46:47], v74 offset:16192\n\t" \
  "s_waitcnt lgkmcnt(4)\n\t" \
  "v_mfma_f32_32x32x16_bf16 v[0:15], v[32:35], v[208:211], v[0:15]\n\t" \
  "v_mfma_f32_32x32x16_bf16 v[16:31], v[36:39], v[208:211], v[16:31]\n\t" \
  "s_waitcnt vmcnt(5)\n\t" \
  "s_barrier\n\t" \
  "ds_read_b64_tr_b16 v[32:33], v74 offset:18432\n\t" \
  "ds_read_b64_tr_b16 v[34:35], v74 offset:19200\n\t" \
  "ds_read_b64_tr_b16 v[36:37], v74 offset:18496\n\t" \
  "ds_read_b64_tr_b16 v[38:39], v74 offset:19264\n\t" \
  "s_waitcnt lgkmcnt(4)\n\t" \
  "v_mfma_f32_32x32x16_bf16 v[0:15], v[40:43], v[212:215], v[0:15]\n\t" \
  "v_mfma_f32_32x32x16_bf16 v[16:31], v[44:47], v[212:215], v[16:31]\n\t" \
  "ds_read_b64_tr_b16 v[40:41], v74 offset:21504\n\t" \
  "ds_read_b64_tr_b16 v[42:43], v74 offset:22272\n\t" \
  "ds_read_b64_tr_b16 v[44:45], v74 offset:21568\n\t" \
  "ds_read_b64_tr_b16 v[46:47], v74 offset:22336\n\t" \
  "s_waitcnt vmcnt(3)\n\t" \
  "ds_read_b64_tr_b16 v[176:177], v84 offset:0\n\t" \
  "ds_read_b64_tr_b16 v[178:179], v84 offset:256\n\t" \
  "ds_read_b64_tr_b16 v[180:181], v84 offset:1024\n\t" \
  "ds_read_b64_tr_b16 v[182:183], v84 offset:1280\n\t" \
  "s_waitcnt lgkmcnt(8)\n\t" \
  "v_mfma_f32_32x32x16_bf16 v[0:15], v[32:35], v[216:219], v[0:15]\n\t" \
  "v_mfma_f32_32x32x16_bf16 v[16:31], v[36:39], v[216:219], v[16:31]\n\t" \
  "v_add_u32 v74, -69632, v74\n\t" \
  "ds_read_b64_tr_b16 v[32:33], v74 offset:34816\n\t" \
  "ds_read_b64_tr_b16 v[34:35], v74 offset:35584\n\t" \
  "ds_read_b64_tr_b16 v[36:37], v74 offset:34880\n\t" \
  "ds_read_b64_tr_b16 v[38:39], v74 offset:35648\n\t" \
  "s_waitcnt lgkmcnt(8)\n\t" \
  "v_mfma_f32_32x32x16_bf16 v[0:15], v[40:43], v[220:223], v[0:15]\n\t" \
  "v_mfma_f32_32x32x16_bf16 v[16:31], v[44:47], v[220:223], v[16:31]\n\t" \
  "v_add_u32 v200, s80, v75\n\t" \
  "v_mov_b32 v201, v85\n\t" \
  "v_mov_b32 v202, v86\n\t" \
  "v_lshl_add_u32 v203, v127, 2, s81\n\t" \
  "v_lshl_add_u32 v204, v127, 2, s82\n\t" \
  "v_mov_b32 v127, v75\n\t" \
  "ds_read_b64_tr_b16 v[40:41], v74 offset:37888\n\t" \
  "ds_read_b64_tr_b16 v[42:43], v74 offset:38656\n\t" \
  "ds_read_b64_tr_b16 v[44:45], v74 offset:37952\n\t" \
  "ds_read_b64_tr_b16 v[46:47], v74 offset:38720\n\t" \
  "s_waitcnt lgkmcnt(4)\n\t" \
  "v_mfma_f32_32x32x16_bf16 v[0:15], v[32:35], v[224:227], v[0:15]\n\t" \
  "v_mfma_f32_32x32x16_bf16 v[16:31], v[36:39], v[224:227], v[16:31]\n\t" \
  "v_and_b32 v205, 0xf0, v75\n\t" \
  "v_lshl_add_u32 v205, v205, 6, s72\n\t" \
  "v_add_u32 v205, 16384, v205\n\t" \
  "v_add_u32 v208, s77, v75\n\t" \
  "v_add_u32 v209, s78, v75\n\t" \
  "ds_read_b64_tr_b16 v[32:33], v74 offset:40960\n\t" \
  "ds_read_b64_tr_b16 v[34:35], v74 offset:41728\n\t" \
  "ds_read_b64_tr_b16 v[36:37], v74 offset:41024\n\t" \
  "ds_read_b64_tr_b16 v[38:39], v74 offset:41792\n\t" \
  "s_waitcnt lgkmcnt(4)\n\t" \
  "v_mfma_f32_32x32x16_bf16 v[0:15], v[40:43], v[228:231], v[0:15]\n\t" \
  "v_mfma_f32_32x32x16_bf16 v[16:31], v[44:47], v[228:231], v[16:31]\n\t" \
  "v_add_u32 v210, 139264, v209\n\t" \
  "v_add_u32 v211, s79, v75\n\t" \
  "ds_read_b64_tr_b16 v[40:41], v74 offset:44032\n\t" \
  "ds_read_b64_tr_b16 v[42:43], v74 offset:44800\n\t" \
  "ds_read_b64_tr_b16 v[44:45], v74 offset:44096\n\t" \
  "ds_read_b64_tr_b16 v[46:47], v74 offset:44864\n\t" \
  "s_waitcnt vmcnt(0)\n\t" \
  "v_mfma_f32_32x32x16_bf16 v[128:143], v[184:187], v[196:199], 0\n\t" \
  "v_mfma_f32_32x32x16_bf16 v[144:159], v[188:191], v[196:199], 0\n\t" \
  "v_mfma_f32_32x32x16_bf16 v[160:175], v[192:195], v[196:199], 0\n\t" \
  "s_barrier\n\t" \
  "global_load_dwordx4 v[110:113], v70, s[66:67] sc1\n\t" \
  "global_load_dwordx4 v[114:117], v70, s[66:67] offset:2560 sc1\n\t" \
  "global_load_dwordx2 v[118:119], v71, s[66:67] sc1\n\t" \
  "s_add_u32 m0, %[w3k], 0\n\t" \
  "s_nop 0\n\t" \
  "global_load_lds_dwordx4 v75, s[42:43] sc1\n\t" \
  "global_load_lds_dwordx4 v75, s[42:43] offset:1024 sc1\n\t" \
  "global_load_lds_dwordx4 v75, s[42:43] offset:2048 sc1\n\t" \
  "s_add_u32 s42, s42, 24576\n\t" \
  "s_addc_u32 s43, s43, 0\n\t" \
  "s_waitcnt lgkmcnt(4)\n\t" \
  "v_mfma_f32_32x32x16_bf16 v[0:15], v[32:35], v[232:235], v[0:15]\n\t" \
  "v_mfma_f32_32x32x16_bf16 v[16:31], v[36:39], v[232:235], v[16:31]\n\t" \
  "ds_read_b64_tr_b16 v[32:33], v74 offset:47104\n\t" \
  "ds_read_b64_tr_b16 v[34:35], v74 offset:47872\n\t" \
  "ds_read_b64_tr_b16 v[36:37], v74 offset:47168\n\t" \
  "ds_read_b64_tr_b16 v[38:39], v74 offset:47936\n\t" \
  "s_add_u32 m0, %[w3k], 24576\n\t" \
  "s_nop 0\n\t" \
  "global_load_lds_dwordx4 v75, s[42:43] sc1\n\t" \
  "global_load_lds_dwordx4 v75, s[42:43] offset:1024 sc1\n\t" \
  "global_load_lds_dwordx4 v75, s[42:43] offset:2048 sc1\n\t" \
  "s_add_u32 s42, s42, 24576\n\t" \
  "s_addc_u32 s43, s43, 0\n\t" \
  "s_waitcnt lgkmcnt(4)\n\t" \
  "v_mfma_f32_32x32x16_bf16 v[0:15], v[40:43], v[236:239], v[0:15]\n\t" \
  "v_mfma_f32_32x32x16_bf16 v[16:31], v[44:47], v[236:239], v[16:31]\n\t" \
  "ds_read_b64_tr_b16 v[40:41], v74 offset:50176\n\t" \
  "ds_read_b64_tr_b16 v[42:43], v74 offset:50944\n\t" \
  "ds_read_b64_tr_b16 v[44:45], v74 offset:50240\n\t" \
  "ds_read_b64_tr_b16 v[46:47], v74 offset:51008\n\t" \
  "s_waitcnt lgkmcnt(4)\n\t" \
  "v_mfma_f32_32x32x16_bf16 v[0:15], v[32:35], v[240:243], v[0:15]\n\t" \
  "v_mfma_f32_32x32x16_bf16 v[16:31], v[36:39], v[240:243], v[16:31]\n\t" \
  "ds_read_b64_tr_b16 v[32:33], v74 offset:53248\n\t" \
  "ds_read_b64_tr_b16 v[34:35], v74 offset:54016\n\t" \
  "ds_read_b64_tr_b16 v[36:37], v74 offset:53312\n\t" \
  "ds_read_b64_tr_b16 v[38:39], v74 offset:54080\n\t" \
  "s_waitcnt lgkmcnt(4)\n\t" \
  "v_mfma_f32_32x32x16_bf16 v[0:15], v[40:43], v[244:247], v[0:15]\n\t" \
  "v_mfma_f32_32x32x16_bf16 v[16:31], v[44:47], v[244:247], v[16:31]\n\t" \
  "ds_read_b64_tr_b16 v[40:41], v74 offset:56320\n\t" \
  "ds_read_b64_tr_b16 v[42:43], v74 offset:57088\n\t" \
  "ds_read_b64_tr_b16 v[44:45], v74 offset:56384\n\t" \
  "ds_read_b64_tr_b16 v[46:47], v74 offset:57152\n\t" \
  "s_waitcnt lgkmcnt(4)\n\t" \
  "v_mfma_f32_32x32x16_bf16 v[0:15], v[32:35], v[248:251], v[0:15]\n\t" \
  "v_mfma_f32_32x32x16_bf16 v[16:31], v[36:39], v[248:251], v[16:31]\n\t" \
  "s_waitcnt lgkmcnt(0)\n\t" \
  "v_mfma_f32_32x32x16_bf16 v[0:15], v[40:43], v[252:255], v[0:15]\n\t" \
  "v_mfma_f32_32x32x16_bf16 v[16:31], v[44:47], v[252:255], v[16:31]\n\t" \
  "s_nop 7\n\t" \
  "s_nop 4\n\t" \
  "s_branch Ea2_%=\n\t" \
  "Sa20_%=:\n\t" \
  "s_bitcmp1_b32 %[fl], 0\n\t" \
  "s_cbranch_scc0 La211_%=\n\t" \
  "global_load_dword v68, v87, %[fb] sc1\n\t" \
  "La211_%=:\n\t" \
  "v_mfma_f32_32x32x16_bf16 v[0:15], v[92:95], v[64:67], 0\n\t" \
  "ds_read_b64_tr_b16 v[40:41], v74 offset:3072\n\t" \
  "ds_read_b64_tr_b16 v[42:43], v74 offset:3840\n\t" \
  "s_waitcnt lgkmcnt(2)\n\t" \
  "v_mfma_f32_32x32x16_bf16 v[0:15], v[32:35], v[96:99], v[0:15]\n\t" \
  "s_bitcmp0_b32 %[fl], 8\n\t" \
  "s_cselect_b32 exec_lo, -1, 0\n\t" \
  "s_bitcmp0_b32 %[fl], 9\n\t" \
  "s_cselect_b32 exec_hi, -1, 0\n\t" \
  "global_store_dwordx4 v76, v[48:51], %[ws] sc1\n\t" \
  "s_bitcmp1_b32 %[fl], 7\n\t" \
  "s_cbranch_scc0 La212_%=\n\t" \
  "s_not_b64 exec, exec\n\t" \
  "s_cbranch_scc0 La212_%=\n\t" \
  "v_mad_i32_i24 v73, v83, -4, v76\n\t" \
  "global_store_dwordx2 v73, v[48:49], %[ws] sc1\n\t" \
  "global_store_dwordx2 v73, v[50:51], s[64:65] sc1\n\t" \
  "La212_%=:\n\t" \
  "s_mov_b64 exec, -1\n\t" \
  "ds_read_b64_tr_b16 v[32:33], v74 offset:6144\n\t" \
  "ds_read_b64_tr_b16 v[34:35], v74 offset:6912\n\t" \
  "s_waitcnt lgkmcnt(2)\n\t" \
  "v_mfma_f32_32x32x16_bf16 v[0:15], v[40:43], v[100:103], v[0:15]\n\t" \
  "ds_read_b64_tr_b16 v[40:41], v74 offset:9216\n\t" \
  "ds_read_b64_tr_b16 v[42:43], v74 offset:9984\n\t" \
  "s_waitcnt lgkmcnt(2)\n\t" \
  "v_mfma_f32_32x32x16_bf16 v[0:15], v[32:35], v[104:107], v[0:15]\n\t" \
  "s_bitcmp0_b32 %[fl], 10\n\t" \
  "s_cselect_b32 exec_lo, -1, 0\n\t" \
  "s_bitcmp0_b32 %[fl], 11\n\t" \
  "s_cselect_b32 exec_hi, -1, 0\n\t" \
  "global_store_dwordx4 v77, v[52:55], %[ws] sc1\n\t" \
  "s_bitcmp1_b32 %[fl], 7\n\t" \
  "s_cbranch_scc0 La213_%=\n\t" \
  "s_not_b64 exec, exec\n\t" \
  "s_cbranch_scc0 La213_%=\n\t" \
  "v_mad_i32_i24 v73, v83, -4, v77\n\t" \
  "global_store_dwordx2 v73, v[52:53], %[ws] sc1\n\t" \
  "global_store_dwordx2 v73, v[54:55], s[64:65] sc1\n\t" \
  "La213_%=:\n\t" \
  "s_mov_b64 exec, -1\n\t" \
  "ds_read_b64_tr_b16 v[32:33], v74 offset:12288\n\t" \
  "ds_read_b64_tr_b16 v[34:35], v74 offset:13056\n\t" \
  "s_waitcnt lgkmcnt(2)\n\t" \
  "v_mfma_f32_32x32x16_bf16 v[0:15], v[40:43], v[108:111], v[0:15]\n\t" \
  "ds_read_b64_tr_b16 v[40:41], v74 offset:15360\n\t" \
  "ds_read_b64_tr_b16 v[42:43], v74 offset:16128\n\t" \
  "s_waitcnt lgkmcnt(2)\n\t" \
  "v_mfma_f32_32x32x16_bf16 v[0:15], v[32:35], v[112:115], v[0:15]\n\t" \
  "s_bitcmp0_b32 %[fl], 12\n\t" \
  "s_cselect_b32 exec_lo, -1, 0\n\t" \
  "s_bitcmp0_b32 %[fl], 13\n\t" \
  "s_cselect_b32 exec_hi, -1, 0\n\t" \
  "global_store_dwordx4 v78, v[56:59], %[ws] sc1\n\t" \
  "s_bitcmp1_b32 %[fl], 7\n\t" \
  "s_cbranch_scc0 La214_%=\n\t" \
  "s_not_b64 exec, exec\n\t" \
  "s_cbranch_scc0 La214_%=\n\t" \
  "v_mad_i32_i24 v73, v83, -4, v78\n\t" \
  "global_store_dwordx2 v73, v[56:57], %[ws] sc1\n\t" \
  "global_store_dwordx2 v73, v[58:59], s[64:65] sc1\n\t" \
  "La214_%=:\n\t" \
  "s_mov_b64 exec, -1\n\t" \
  "ds_read_b64_tr_b16 v[32:33], v74 offset:18432\n\t" \
  "ds_read_b64_tr_b16 v[34:35], v74 offset:19200\n\t" \
  "s_waitcnt lgkmcnt(2)\n\t" \
  "v_mfma_f32_32x32x16_bf16 v[0:15], v[40:43], v[116:119], v[0:15]\n\t" \
  "ds_read_b64_tr_b16 v[40:41], v74 offset:21504\n\t" \
  "ds_read_b64_tr_b16 v[42:43], v74 offset:22272\n\t" \
  "s_waitcnt lgkmcnt(2)\n\t" \
  "v_mfma_f32_32x32x16_bf16 v[0:15], v[32:35], v[120:123], v[0:15]\n\t" \
  "s_bitcmp0_b32 %[fl], 14\n\t" \
  "s_cselect_b32 exec_lo, -1, 0\n\t" \
  "s_bitcmp0_b32 %[fl], 15\n\t" \
  "s_cselect_b32 exec_hi, -1, 0\n\t" \
  "global_store_dwordx4 v79, v[60:63], %[ws] sc1\n\t" \
  "s_bitcmp1_b32 %[fl], 7\n\t" \
  "s_cbranch_scc0 La215_%=\n\t" \
  "s_not_b64 exec, exec\n\t" \
  "s_cbranch_scc0 La215_%=\n\t" \
  "v_mad_i32_i24 v73, v83, -4, v79\n\t" \
  "global_store_dwordx2 v73, v[60:61], %[ws] sc1\n\t" \
  "global_store_dwordx2 v73, v[62:63], s[64:65] sc1\n\t" \
  "La215_%=:\n\t" \
  "s_mov_b64 exec, -1\n\t" \
  "s_waitcnt vmcnt(4)\n\t" \
  "s_waitcnt lgkmcnt(0)\n\t" \
  "s_barrier\n\t" \
  "ds_read_b64_tr_b16 v[32:33], v74 offset:34816\n\t" \
  "ds_read_b64_tr_b16 v[34:35], v74 offset:35584\n\t" \
  "v_mfma_f32_32x32x16_bf16 v[0:15], v[40:43], v[124:127], v[0:15]\n\t" \
  "ds_read_b64_tr_b16 v[40:41], v74 offset:37888\n\t" \
  "ds_read_b64_tr_b16 v[42:43], v74 offset:38656\n\t" \
  "s_waitcnt lgkmcnt(2)\n\t" \
  "v_mfma_f32_32x32x16_bf16 v[0:15], v[32:35], v[128:131], v[0:15]\n\t" \
  "s_add_u32 m0, %[w3k], 104448\n\t" \
  "s_nop 0\n\t" \
  "global_load_lds_dwordx4 v80, s[44:45]\n\t" \
  "ds_read_b64_tr_b16 v[32:33], v74 offset:40960\n\t" \
  "ds_read_b64_tr_b16 v[34:35], v74 offset:41728\n\t" \
  "s_waitcnt lgkmcnt(2)\n\t" \
  "v_mfma_f32_32x32x16_bf16 v[0:15], v[40:43], v[132:135], v[0:15]\n\t" \
  "v_mov_b32 v120, 0xdddddddd\n\t" \
  "v_add_u32 v121, 2688, v75\n\t" \
  "v_bfe_u32 v127, v75, 4, 5\n\t" \
  "v_lshlrev_b32 v122, 3, v127\n\t" \
  "v_add_u32 v122, 3712, v122\n\t" \
  "global_load_lds_dwordx4 v81, s[44:45] offset:1024\n\t" \
  "ds_read_b64_tr_b16 v[40:41], v74 offset:44032\n\t" \
  "ds_read_b64_tr_b16 v[42:43], v74 offset:44800\n\t" \
  "s_waitcnt lgkmcnt(2)\n\t" \
  "v_mfma_f32_32x32x16_bf16 v[0:15], v[32:35], v[136:139], v[0:15]\n\t" \
  "v_lshrrev_b32 v123, 9, v75\n\t" \
  "v_mul_u32_u24 v123, 192, v123\n\t" \
  "v_sub_u32 v123, v75, v123\n\t" \
  "v_cmp_lt_u32 vcc, 27, v127\n\t" \
  "v_subrev_u32 v126, 192, v123\n\t" \
  "v_subrev_u32 v125, 20, v127\n\t" \
  "global_load_lds_dwordx4 v82, s[44:45] offset:2048\n\t" \
  "v_lshrrev_b32 v72, 6, v75\n\t" \
  "v_mul_u32_u24 v72, 1280, v72\n\t" \
  "v_and_or_b32 v72, v75, 48, v72\n\t" \
  "s_add_u32 m0, %[w2k], 0\n\t" \
  "s_nop 0\n\t" \
  "global_load_lds_dwordx4 v72, s[60:61]\n\t" \
  "s_add_u32 m0, %[w2k], 1024\n\t" \
  "s_nop 0\n\t" \
  "global_load_lds_dwordx4 v72, s[62:63]\n\t" \
  "ds_read_b64_tr_b16 v[32:33], v74 offset:47104\n\t" \
  "ds_read_b64_tr_b16 v[34:35], v74 offset:47872\n\t" \
  "s_waitcnt lgkmcnt(2)\n\t" \
  "v_mfma_f32_32x32x16_bf16 v[0:15], v[40:43], v[140:143], v[0:15]\n\t" \
  "ds_read_b64_tr_b16 v[40:41], v74 offset:50176\n\t" \
  "ds_read_b64_tr_b16 v[42:43], v74 offset:50944\n\t" \
  "s_waitcnt lgkmcnt(2)\n\t" \
  "v_mfma_f32_32x32x16_bf16 v[0:15], v[32:35], v[144:147], v[0:15]\n\t" \
  "v_cndmask_b32 v123, v123, v126, vcc\n\t" \
  "v_cmp_gt_u32 vcc, 8, v125\n\t" \
  "v_min_u32 v125, 1, v125\n\t" \
  "v_lshlrev_b32 v125, 4, v125\n\t" \
  "v_add_u32 v125, 640, v125\n\t" \
  "ds_read_b64_tr_b16 v[32:33], v74 offset:53248\n\t" \
  "ds_read_b64_tr_b16 v[34:35], v74 offset:54016\n\t" \
  "s_waitcnt lgkmcnt(2)\n\t" \
  "v_mfma_f32_32x32x16_bf16 v[0:15], v[40:43], v[148:151], v[0:15]\n\t" \
  "v_cndmask_b32 v123, v123, v125, vcc\n\t" \
  "v_add_u32 v123, 3968, v123\n\t" \
  "v_add_u32 v124, %[w3k], v75\n\t" \
  "v_add_u32 v124, 103424, v124\n\t" \
  "v_add_u32 v125, -12288, v124\n\t" \
  "ds_read_b64_tr_b16 v[40:41], v74 offset:56320\n\t" \
  "ds_read_b64_tr_b16 v[42:43], v74 offset:57088\n\t" \
  "s_waitcnt lgkmcnt(2)\n\t" \
  "v_mfma_f32_32x32x16_bf16 v[0:15], v[32:35], v[152:155], v[0:15]\n\t" \
  "v_add_u32 v126, s73, v75\n\t" \
  "s_mov_b64 s[40:41], %[ws]\n\t" \
  "s_add_u32 s44, s40, s83\n\t" \
  "s_addc_u32 s45, s41, 0\n\t" \
  "s_mul_i32 s50, %[w2k], 5\n\t" \
  "s_add_u32 s50, s50, 21504\n\t" \
  "v_add_u32 v74, 69632, v74\n\t" \
  "ds_read_b64_tr_b16 v[32:33], v74 offset:0\n\t" \
  "ds_read_b64_tr_b16 v[34:35], v74 offset:768\n\t" \
  "s_waitcnt lgkmcnt(2)\n\t" \
  "v_mfma_f32_32x32x16_bf16 v[0:15], v[40:43], v[156:159], v[0:15]\n\t" \
  "s_waitcnt vmcnt(5)\n\t" \
  "s_barrier\n\t" \
  "s_bitcmp1_b32 %[fl], 0\n\t" \
  "s_cbranch_scc0 La216_%=\n\t" \
  "s_bitcmp1_b32 %[fl], 1\n\t" \
  "s_cbranch_scc1 La217_%=\n\t" \
  "s_mov_b32 %[cnt], 0x100000\n\t" \
  "La218_%=:\n\t" \
  "v_cmp_ne_u32 vcc, v88, v68\n\t" \
  "s_cbranch_vccnz La217_%=\n\t" \
  "global_load_dword v68, v87, %[fb] sc1\n\t" \
  "s_sub_u32 %[cnt], %[cnt], 1\n\t" \
  "s_waitcnt vmcnt(0)\n\t" \
  "s_cbranch_scc0 La218_%=\n\t" \
  "La217_%=:\n\t" \
  "v_lshl_add_u32 v69, v75, 6, s72\n\t" \
  "v_mov_b32 v68, 1\n\t" \
  "s_mov_b64 exec, 0xffff\n\t" \
  "global_store_dword v69, v68, %[fb] sc1\n\t" \
  "s_mov_b64 exec, -1\n\t" \
  "La216_%=:\n\t" \
  "s_bitcmp1_b32 %[fl], 1\n\t" \
  "s_cbranch_scc0 La219_%=\n\t" \
  "v_add_u32 v89, 1, v89\n\t" \
  "global_store_dword v87, v89, %[fb] sc1\n\t" \
  "La219_%=:\n\t" \
  "ds_read_b64_tr_b16 v[40:41], v74 offset:3072\n\t" \
  "ds_read_b64_tr_b16 v[42:43], v74 offset:3840\n\t" \
  "s_waitcnt lgkmcnt(2)\n\t" \
  "v_mfma_f32_32x32x16_bf16 v[0:15], v[32:35], v[160:163], v[0:15]\n\t" \
  "ds_read_b64_tr_b16 v[32:33], v74 offset:6144\n\t" \
  "ds_read_b64_tr_b16 v[34:35], v74 offset:6912\n\t" \
  "s_waitcnt lgkmcnt(2)\n\t" \
  "v_mfma_f32_32x32x16_bf16 v[0:15], v[40:43], v[164:167], v[0:15]\n\t" \
  "ds_read_b64_tr_b16 v[40:41], v74 offset:9216\n\t" \
  "ds_read_b64_tr_b16 v[42:43], v74 offset:9984\n\t" \
  "s_waitcnt lgkmcnt(2)\n\t" \
  "v_mfma_f32_32x32x16_bf16 v[0:15], v[32:35], v[168:171], v[0:15]\n\t" \
  "ds_read_b64_tr_b16 v[32:33], v74 offset:12288\n\t" \
  "ds_read_b64_tr_b16 v[34:35], v74 offset:13056\n\t" \
  "s_waitcnt lgkmcnt(2)\n\t" \
  "v_mfma_f32_32x32x16_bf16 v[0:15], v[40:43], v[172:175], v[0:15]\n\t" \
  "ds_read_b64_tr_b16 v[40:41], v74 offset:15360\n\t" \
  "ds_read_b64_tr_b16 v[42:43], v74 offset:16128\n\t" \
  "s_waitcnt lgkmcnt(2)\n\t" \
  "v_mfma_f32_32x32x16_bf16 v[0:15], v[32:35], v[176:179], v[0:15]\n\t" \
  "ds_read_b64_tr_b16 v[32:33], v74 offset:18432\n\t" \
  "ds_read_b64_tr_b16 v[34:35], v74 offset:19200\n\t" \
  "s_waitcnt lgkmcnt(2)\n\t" \
  "v_mfma_f32_32x32x16_bf16 v[0:15], v[40:43], v[180:183], v[0:15]\n\t" \
  "ds_read_b64_tr_b16 v[40:41], v74 offset:21504\n\t" \
  "ds_read_b64_tr_b16 v[42:43], v74 offset:22272\n\t" \
  "s_waitcnt lgkmcnt(2)\n\t" \
  "v_mfma_f32_32x32x16_bf16 v[0:15], v[32:35], v[184:187], v[0:15]\n\t" \
  "v_add_u32 v74, 69632, v74\n\t" \
  "ds_read_b64_tr_b16 v[32:33], v74 offset:0\n\t" \
  "ds_read_b64_tr_b16 v[34:35], v74 offset:768\n\t" \
  "s_waitcnt lgkmcnt(2)\n\t" \
  "v_mfma_f32_32x32x16_bf16 v[0:15], v[40:43], v[188:191], v[0:15]\n\t" \
  "ds_read_b64_tr_b16 v[40:41], v74 offset:3072\n\t" \
  "ds_read_b64_tr_b16 v[42:43], v74 offset:3840\n\t" \
  "s_waitcnt lgkmcnt(2)\n\t" \
  "v_mfma_f32_32x32x16_bf16 v[0:15], v[32:35], v[192:195], v[0:15]\n\t" \
  "ds_read_b64_tr_b16 v[32:33], v74 offset:6144\n\t" \
  "ds_read_b64_tr_b16 v[34:35], v74 offset:6912\n\t" \
  "s_waitcnt lgkmcnt(2)\n\t" \
  "v_mfma_f32_32x32x16_bf16 v[0:15], v[40:43], v[196:199], v[0:15]\n\t" \
  "ds_read_b64_tr_b16 v[40:41], v74 offset:9216\n\t" \
  "ds_read_b64_tr_b16 v[42:43], v74 offset:9984\n\t" \
  "s_waitcnt lgkmcnt(2)\n\t" \
  "v_mfma_f32_32x32x16_bf16 v[0:15], v[32:35], v[200:203], v[0:15]\n\t" \
  "ds_read_b64_tr_b16 v[32:33], v74 offset:12288\n\t" \
  "ds_read_b64_tr_b16 v[34:35], v74 offset:13056\n\t" \
  "v_mov_b64 v[184:185], 0\n\t" \
  "v_mov_b64 v[186:187], 0\n\t" \
  "v_mov_b64 v[188:189], 0\n\t" \
  "v_mov_b64 v[190:191], 0\n\t" \
  "v_mov_b64 v[192:193], 0\n\t" \
  "v_mov_b64 v[194:195], 0\n\t" \
  "v_mov_b64 v[196:197], 0\n\t" \
  "v_mov_b64 v[198:199], 0\n\t" \
  "s_bitcmp1_b32 %[fl], 17\n\t" \
  "s_cselect_b32 %[cnt], -1, 0\n\t" \
  "global_load_ushort v184, v83, s[68:69]\n\t" \
  "global_load_ushort v188, v83, s[68:69] offset:64\n\t" \
  "v_and_b32 v196, %[cnt], v64\n\t" \
  "v_and_b32 v71, 0x200, v75\n\t" \
  "v_add_u32 v70, s74, v75\n\t" \
  "v_lshl_add_u32 v71, v71, 2, v75\n\t" \
  "v_add_u32 v71, s75, v71\n\t" \
  "s_waitcnt lgkmcnt(2)\n\t" \
  "v_mfma_f32_32x32x16_bf16 v[0:15], v[40:43], v[204:207], v[0:15]\n\t" \
  "ds_read_b64_tr_b16 v[40:41], v74 offset:15360\n\t" \
  "ds_read_b64_tr_b16 v[42:43], v74 offset:16128\n\t" \
  "s_waitcnt lgkmcnt(2)\n\t" \
  "v_mfma_f32_32x32x16_bf16 v[0:15], v[32:35], v[208:211], v[0:15]\n\t" \
  "s_waitcnt vmcnt(4)\n\t" \
  "s_barrier\n\t" \
  "ds_read_b64_tr_b16 v[32:33], v74 offset:18432\n\t" \
  "ds_read_b64_tr_b16 v[34:35], v74 offset:19200\n\t" \
  "s_waitcnt lgkmcnt(2)\n\t" \
  "v_mfma_f32_32x32x16_bf16 v[0:15], v[40:43], v[212:215], v[0:15]\n\t" \
  "ds_read_b64_tr_b16 v[40:41], v74 offset:21504\n\t" \
  "ds_read_b64_tr_b16 v[42:43], v74 offset:22272\n\t" \
  "s_waitcnt vmcnt(2)\n\t" \
  "ds_read_b64_tr_b16 v[176:177], v84 offset:0\n\t" \
  "ds_read_b64_tr_b16 v[178:179], v84 offset:256\n\t" \
  "ds_read_b64_tr_b16 v[180:181], v84 offset:1024\n\t" \
  "ds_read_b64_tr_b16 v[182:183], v84 offset:1280\n\t" \
  "s_waitcnt lgkmcnt(6)\n\t" \
  "v_mfma_f32_32x32x16_bf16 v[0:15], v[32:35], v[216:219], v[0:15]\n\t" \
  "v_add_u32 v74, -69632, v74\n\t" \
  "ds_read_b64_tr_b16 v[32:33], v74 offset:34816\n\t" \
  "ds_read_b64_tr_b16 v[34:35], v74 offset:35584\n\t" \
  "s_bitcmp1_b32 %[fl], 0\n\t" \
  "s_cbranch_scc0 La2110_%=\n\t" \
  "v_add_u32 v69, s73, v75\n\t" \
  "global_load_dwordx4 v[56:59], v69, %[fb] sc1\n\t" \
  "La2110_%=:\n\t" \
  "s_waitcnt lgkmcnt(6)\n\t" \
  "v_mfma_f32_32x32x16_bf16 v[0:15], v[40:43], v[220:223], v[0:15]\n\t" \
  "v_add_u32 v200, s80, v75\n\t" \
  "v_mov_b32 v201, v85\n\t" \
  "v_mov_b32 v202, v86\n\t" \
  "v_lshl_add_u32 v203, v127, 2, s81\n\t" \
  "v_lshl_add_u32 v204, v127, 2, s82\n\t" \
  "v_mov_b32 v127, v75\n\t" \
  "ds_read_b64_tr_b16 v[40:41], v74 offset:37888\n\t" \
  "ds_read_b64_tr_b16 v[42:43], v74 offset:38656\n\t" \
  "s_waitcnt lgkmcnt(2)\n\t" \
  "v_mfma_f32_32x32x16_bf16 v[0:15], v[32:35], v[224:227], v[0:15]\n\t" \
  "v_and_b32 v205, 0xf0, v75\n\t" \
  "v_lshl_add_u32 v205, v205, 6, s72\n\t" \
  "v_add_u32 v205, 16384, v205\n\t" \
  "v_add_u32 v208, s77, v75\n\t" \
  "v_add_u32 v209, s78, v75\n\t" \
  "ds_read_b64_tr_b16 v[32:33], v74 offset:40960\n\t" \
  "ds_read_b64_tr_b16 v[34:35], v74 offset:41728\n\t" \
  "s_waitcnt lgkmcnt(2)\n\t" \
  "v_mfma_f32_32x32x16_bf16 v[0:15], v[40:43], v[228:231], v[0:15]\n\t" \
  "v_add_u32 v210, 139264, v209\n\t" \
  "v_add_u32 v211, s79, v75\n\t" \
  "ds_read_b64_tr_b16 v[40:41], v74 offset:44032\n\t" \
  "ds_read_b64_tr_b16 v[42:43], v74 offset:44800\n\t" \
  "s_waitcnt vmcnt(0)\n\t" \
  "v_mfma_f32_32x32x16_bf16 v[128:143], v[184:187], v[196:199], 0\n\t" \
  "v_mfma_f32_32x32x16_bf16 v[144:159], v[188:191], v[196:199], 0\n\t" \
  "s_bitcmp1_b32 %[fl], 0\n\t" \
  "s_cbranch_scc0 La2111_%=\n\t" \
  "s_mov_b32 %[cnt], 0x100000\n\t" \
  "F1a2113_%=:\n\t" \
  "s_waitcnt vmcnt(0)\n\t" \
  "v_and_b32 v56, v56, v57\n\t" \
  "v_and_b32 v58, v58, v59\n\t" \
  "v_and_b32 v56, v56, v58\n\t" \
  "v_cmp_ne_u32 vcc, 1, v56\n\t" \
  "s_cbranch_vccz F1a2112_%=\n\t" \
  "s_bitcmp1_b32 %[fl], 1\n\t" \
  "s_cbranch_scc0 La2114_%=\n\t" \
  "v_add_u32 v89, 1, v89\n\t" \
  "global_store_dword v87, v89, %[fb] sc1\n\t" \
  "La2114_%=:\n\t" \
  "global_load_dwordx4 v[56:59], v69, %[fb] sc1\n\t" \
  "s_sub_u32 %[cnt], %[cnt], 1\n\t" \
  "s_cbranch_scc0 F1a2113_%=\n\t" \
  "s_waitcnt vmcnt(0)\n\t" \
  "F1a2112_%=:\n\t" \
  "La2111_%=:\n\t" \
  "s_barrier\n\t" \
  "global_load_dwordx4 v[110:113], v70, s[66:67] sc1\n\t" \
  "global_load_dwordx4 v[114:117], v70, s[66:67] offset:2560 sc1\n\t" \
  "global_load_dwordx2 v[118:119], v71, s[66:67] sc1\n\t" \
  "s_add_u32 m0, %[w3k], 0\n\t" \
  "s_nop 0\n\t" \
  "global_load_lds_dwordx4 v75, s[42:43] sc1\n\t" \
  "global_load_lds_dwordx4 v75, s[42:43] offset:1024 sc1\n\t" \
  "global_load_lds_dwordx4 v75, s[42:43] offset:2048 sc1\n\t" \
  "s_add_u32 s42, s42, 24576\n\t" \
  "s_addc_u32 s43, s43, 0\n\t" \
  "s_waitcnt lgkmcnt(2)\n\t" \
  "v_mfma_f32_32x32x16_bf16 v[0:15], v[32:35], v[232:235], v[0:15]\n\t" \
  "ds_read_b64_tr_b16 v[32:33], v74 offset:47104\n\t" \
  "ds_read_b64_tr_b16 v[34:35], v74 offset:47872\n\t" \
  "s_add_u32 m0, %[w3k], 24576\n\t" \
  "s_nop 0\n\t" \
  "global_load_lds_dwordx4 v75, s[42:43] sc1\n\t" \
  "global_load_lds_dwordx4 v75, s[42:43] offset:1024 sc1\n\t" \
  "global_load_lds_dwordx4 v75, s[42:43] offset:2048 sc1\n\t" \
  "s_add_u32 s42, s42, 24576\n\t" \
  "s_addc_u32 s43, s43, 0\n\t" \
  "s_waitcnt lgkmcnt(2)\n\t" \
  "v_mfma_f32_32x32x16_bf16 v[0:15], v[40:43], v[236:239], v[0:15]\n\t" \
  "ds_read_b64_tr_b16 v[40:41], v74 offset:50176\n\t" \
  "ds_read_b64_tr_b16 v[42:43], v74 offset:50944\n\t" \
  "s_waitcnt lgkmcnt(2)\n\t" \
  "v_mfma_f32_32x32x16_bf16 v[0:15], v[32:35], v[240:243], v[0:15]\n\t" \
  "ds_read_b64_tr_b16 v[32:33], v74 offset:53248\n\t" \
  "ds_read_b64_tr_b16 v[34:35], v74 offset:54016\n\t" \
  "s_waitcnt lgkmcnt(2)\n\t" \
  "v_mfma_f32_32x32x16_bf16 v[0:15], v[40:43], v[244:247], v[0:15]\n\t" \
  "ds_read_b64_tr_b16 v[40:41], v74 offset:56320\n\t" \
  "ds_read_b64_tr_b16 v[42:43], v74 offset:57088\n\t" \
  "s_waitcnt lgkmcnt(2)\n\t" \
  "v_mfma_f32_32x32x16_bf16 v[0:15], v[32:35], v[248:251], v[0:15]\n\t" \
  "s_waitcnt lgkmcnt(0)\n\t" \
  "v_mfma_f32_32x32x16_bf16 v[0:15], v[40:43], v[252:255], v[0:15]\n\t" \
  "s_nop 7\n\t" \
  "s_nop 4\n\t" \
  "Ea2_%=:\n\t"
#define A3 \
  "s_nop 4\n\t" \
  "s_bitcmp1_b32 %[fl], 2\n\t" \
  "s_cbranch_scc1 Sa30_%=\n\t" \
  "global_load_dword v212, v203, s[40:41] sc1\n\t" \
  "global_load_dword v213, v204, s[40:41] sc1\n\t" \
  "v_mov_b64 v[64:65], 0\n\t" \
  "v_mov_b64 v[66:67], 0\n\t" \
  "v_mov_b64 v[68:69], 0\n\t" \
  "v_mov_b64 v[70:71], 0\n\t" \
  "v_mov_b64 v[72:73], 0\n\t" \
  "v_mov_b64 v[74:75], 0\n\t" \
  "v_mov_b64 v[76:77], 0\n\t" \
  "v_mov_b64 v[78:79], 0\n\t" \
  "v_mov_b64 v[80:81], 0\n\t" \
  "v_mov_b64 v[82:83], 0\n\t" \
  "v_mov_b64 v[84:85], 0\n\t" \
  "v_mov_b64 v[86:87], 0\n\t" \
  "v_mov_b64 v[88:89], 0\n\t" \
  "v_mov_b64 v[90:91], 0\n\t" \
  "v_mov_b64 v[92:93], 0\n\t" \
  "v_mov_b64 v[94:95], 0\n\t" \
  "s_waitcnt vmcnt(5)\n\t" \
  "s_setprio 3\n\t" \
  "s_barrier\n\t" \
  "ds_read_b128 v[104:107], v121\n\t" \
  "ds_read_b64 v[108:109], v122\n\t" \
  "ds_read_b128 v[96:99], v123\n\t" \
  "ds_read_b128 v[100:103], v123 offset:704\n\t" \
  "v_mul_f32 v0, v0, |v0|\n\t" \
  "v_mul_f32 v1, v1, |v1|\n\t" \
  "v_cvt_pk_bf16_f32 v216, v0, v1\n\t" \
  "v_pk_max_i16 v216, v216, 0\n\t" \
  "v_mul_f32 v2, v2, |v2|\n\t" \
  "v_mul_f32 v3, v3, |v3|\n\t" \
  "v_cvt_pk_bf16_f32 v217, v2, v3\n\t" \
  "v_pk_max_i16 v217, v217, 0\n\t" \
  "v_mul_f32 v4, v4, |v4|\n\t" \
  "v_mul_f32 v5, v5, |v5|\n\t" \
  "v_cvt_pk_bf16_f32 v218, v4, v5\n\t" \
  "v_pk_max_i16 v218, v218, 0\n\t" \
  "v_mul_f32 v6, v6, |v6|\n\t" \
  "v_mul_f32 v7, v7, |v7|\n\t" \
  "v_cvt_pk_bf16_f32 v219, v6, v7\n\t" \
  "v_pk_max_i16 v219, v219, 0\n\t" \
  "v_mul_f32 v8, v8, |v8|\n\t" \
  "v_mul_f32 v9, v9, |v9|\n\t" \
  "v_cvt_pk_bf16_f32 v220, v8, v9\n\t" \
  "v_pk_max_i16 v220, v220, 0\n\t" \
  "v_mul_f32 v10, v10, |v10|\n\t" \
  "v_mul_f32 v11, v11, |v11|\n\t" \
  "v_cvt_pk_bf16_f32 v221, v10, v11\n\t" \
  "v_pk_max_i16 v221, v221, 0\n\t" \
  "v_mul_f32 v12, v12, |v12|\n\t" \
  "v_mul_f32 v13, v13, |v13|\n\t" \
  "v_cvt_pk_bf16_f32 v222, v12, v13\n\t" \
  "v_pk_max_i16 v222, v222, 0\n\t" \
  "v_mul_f32 v14, v14, |v14|\n\t" \
  "v_mul_f32 v15, v15, |v15|\n\t" \
  "v_cvt_pk_bf16_f32 v223, v14, v15\n\t" \
  "v_pk_max_i16 v223, v223, 0\n\t" \
  "s_waitcnt lgkmcnt(3)\n\t" \
  "v_mfma_f32_32x32x16_bf16 v[32:47], v[104:107], v[110:113], 0\n\t" \
  "v_mfma_f32_32x32x16_bf16 v[48:63], v[104:107], v[114:117], 0\n\t" \
  "ds_read_b128 v[104:107], v121 offset:5376\n\t" \
  "s_waitcnt lgkmcnt(3)\n\t" \
  "v_mul_f32 v16, v16, |v16|\n\t" \
  "v_mul_f32 v17, v17, |v17|\n\t" \
  "v_cvt_pk_bf16_f32 v224, v16, v17\n\t" \
  "v_pk_max_i16 v224, v224, 0\n\t" \
  "v_mul_f32 v18, v18, |v18|\n\t" \
  "v_mul_f32 v19, v19, |v19|\n\t" \
  "v_cvt_pk_bf16_f32 v225, v18, v19\n\t" \
  "v_pk_max_i16 v225, v225, 0\n\t" \
  "v_mul_f32 v20, v20, |v20|\n\t" \
  "v_mul_f32 v21, v21, |v21|\n\t" \
  "v_cvt_pk_bf16_f32 v226, v20, v21\n\t" \
  "v_pk_max_i16 v226, v226, 0\n\t" \
  "v_mfma_f32_32x32x4_2b_bf16 v[32:63], v[108:109], v[118:119], v[32:63]\n\t" \
  "ds_read_b64 v[108:109], v122 offset:5376\n\t" \
  "v_mul_f32 v22, v22, |v22|\n\t" \
  "v_mul_f32 v23, v23, |v23|\n\t" \
  "v_cvt_pk_bf16_f32 v227, v22, v23\n\t" \
  "v_pk_max_i16 v227, v227, 0\n\t" \
  "v_mul_f32 v24, v24, |v24|\n\t" \
  "v_mul_f32 v25, v25, |v25|\n\t" \
  "v_cvt_pk_bf16_f32 v228, v24, v25\n\t" \
  "v_pk_max_i16 v228, v228, 0\n\t" \
  "v_mul_f32 v26, v26, |v26|\n\t" \
  "v_mul_f32 v27, v27, |v27|\n\t" \
  "v_cvt_pk_bf16_f32 v229, v26, v27\n\t" \
  "v_pk_max_i16 v229, v229, 0\n\t" \
  "v_mul_f32 v28, v28, |v28|\n\t" \
  "v_mul_f32 v29, v29, |v29|\n\t" \
  "v_cvt_pk_bf16_f32 v230, v28, v29\n\t" \
  "v_pk_max_i16 v230, v230, 0\n\t" \
  "v_mul_f32 v30, v30, |v30|\n\t" \
  "v_mul_f32 v31, v31, |v31|\n\t" \
  "v_cvt_pk_bf16_f32 v231, v30, v31\n\t" \
  "v_pk_max_i16 v231, v231, 0\n\t" \
  "global_store_dwordx4 v200, v[216:219], s[40:41] sc1\n\t" \
  "global_store_dwordx4 v200, v[220:223], s[40:41] offset:1024 sc1\n\t" \
  "s_setprio 2\n\t" \
  "v_exp_f32 v32, v32\n\t" \
  "v_exp_f32 v33, v33\n\t" \
  "v_exp_f32 v34, v34\n\t" \
  "v_exp_f32 v35, v35\n\t" \
  "v_exp_f32 v36, v36\n\t" \
  "v_exp_f32 v37, v37\n\t" \
  "v_exp_f32 v38, v38\n\t" \
  "v_exp_f32 v39, v39\n\t" \
  "v_exp_f32 v48, v48\n\t" \
  "v_exp_f32 v49, v49\n\t" \
  "v_exp_f32 v50, v50\n\t" \
  "v_exp_f32 v51, v51\n\t" \
  "v_exp_f32 v52, v52\n\t" \
  "v_exp_f32 v53, v53\n\t" \
  "v_exp_f32 v54, v54\n\t" \
  "v_exp_f32 v55, v55\n\t" \
  "v_exp_f32 v40, v40\n\t" \
  "v_exp_f32 v41, v41\n\t" \
  "v_exp_f32 v42, v42\n\t" \
  "v_exp_f32 v43, v43\n\t" \
  "v_exp_f32 v44, v44\n\t" \
  "v_exp_f32 v45, v45\n\t" \
  "v_exp_f32 v46, v46\n\t" \
  "v_exp_f32 v47, v47\n\t" \
  "s_setprio 2\n\t" \
  "v_exp_f32 v56, v56\n\t" \
  "v_exp_f32 v57, v57\n\t" \
  "v_exp_f32 v58, v58\n\t" \
  "v_exp_f32 v59, v59\n\t" \
  "v_exp_f32 v60, v60\n\t" \
  "v_exp_f32 v61, v61\n\t" \
  "v_exp_f32 v62, v62\n\t" \
  "v_exp_f32 v63, v63\n\t" \
  "s_setprio 3\n\t" \
  "s_waitcnt lgkmcnt(0)\n\t" \
  "v_mfma_f32_32x32x16_bf16 v[0:15], v[104:107], v[110:113], 0\n\t" \
  "v_mfma_f32_32x32x16_bf16 v[16:31], v[104:107], v[114:117], 0\n\t" \
  "ds_read_b128 v[104:107], v121 offset:10752\n\t" \
  "v_smfmac_f32_32x32x32_bf16 v[64:79], v[96:99], v[32:39], v120\n\t" \
  "v_smfmac_f32_32x32x32_bf16 v[80:95], v[96:99], v[48:55], v120\n\t" \
  "ds_read_b128 v[96:99], v123 offset:5376\n\t" \
  "v_mfma_f32_32x32x4_2b_bf16 v[0:31], v[108:109], v[118:119], v[0:31]\n\t" \
  "ds_read_b64 v[108:109], v122 offset:10752\n\t" \
  "v_smfmac_f32_32x32x32_bf16 v[64:79], v[100:103], v[40:47], v120\n\t" \
  "v_smfmac_f32_32x32x32_bf16 v[80:95], v[100:103], v[56:63], v120\n\t" \
  "ds_read_b128 v[100:103], v123 offset:6080\n\t" \
  "s_add_u32 m0, %[w3k], 49152\n\t" \
  "s_nop 0\n\t" \
  "global_load_lds_dwordx4 v127, s[42:43] sc1\n\t" \
  "global_load_lds_dwordx4 v127, s[42:43] offset:1024 sc1\n\t" \
  "global_load_lds_dwordx4 v127, s[42:43] offset:2048 sc1\n\t" \
  "s_add_u32 s42, s42, 24576\n\t" \
  "s_addc_u32 s43, s43, 0\n\t" \
  "global_store_dwordx4 v200, v[224:227], s[40:41] offset:2048 sc1\n\t" \
  "global_store_dwordx4 v200, v[228:231], s[40:41] offset:3072 sc1\n\t" \
  "s_setprio 0\n\t" \
  "v_exp_f32 v0, v0\n\t" \
  "v_exp_f32 v1, v1\n\t" \
  "v_exp_f32 v2, v2\n\t" \
  "v_exp_f32 v3, v3\n\t" \
  "v_exp_f32 v4, v4\n\t" \
  "v_exp_f32 v5, v5\n\t" \
  "v_exp_f32 v6, v6\n\t" \
  "v_exp_f32 v7, v7\n\t" \
  "v_exp_f32 v16, v16\n\t" \
  "v_exp_f32 v17, v17\n\t" \
  "v_exp_f32 v18, v18\n\t" \
  "v_exp_f32 v19, v19\n\t" \
  "v_exp_f32 v20, v20\n\t" \
  "v_exp_f32 v21, v21\n\t" \
  "v_exp_f32 v22, v22\n\t" \
  "v_exp_f32 v23, v23\n\t" \
  "v_exp_f32 v8, v8\n\t" \
  "v_exp_f32 v9, v9\n\t" \
  "v_exp_f32 v10, v10\n\t" \
  "v_exp_f32 v11, v11\n\t" \
  "v_exp_f32 v12, v12\n\t" \
  "v_exp_f32 v13, v13\n\t" \
  "v_exp_f32 v14, v14\n\t" \
  "v_exp_f32 v15, v15\n\t" \
  "s_setprio 1\n\t" \
  "v_exp_f32 v24, v24\n\t" \
  "v_exp_f32 v25, v25\n\t" \
  "v_exp_f32 v26, v26\n\t" \
  "v_exp_f32 v27, v27\n\t" \
  "v_exp_f32 v28, v28\n\t" \
  "v_exp_f32 v29, v29\n\t" \
  "v_exp_f32 v30, v30\n\t" \
  "v_exp_f32 v31, v31\n\t" \
  "s_setprio 3\n\t" \
  "s_waitcnt lgkmcnt(0)\n\t" \
  "v_mfma_f32_32x32x16_bf16 v[32:47], v[104:107], v[110:113], 0\n\t" \
  "v_mfma_f32_32x32x16_bf16 v[48:63], v[104:107], v[114:117], 0\n\t" \
  "ds_read_b128 v[104:107], v121 offset:16128\n\t" \
  "v_smfmac_f32_32x32x32_bf16 v[64:79], v[96:99], v[0:7], v120\n\t" \
  "v_smfmac_f32_32x32x32_bf16 v[80:95], v[96:99], v[16:23], v120\n\t" \
  "ds_read_b128 v[96:99], v123 offset:10752\n\t" \
  "v_mfma_f32_32x32x4_2b_bf16 v[32:63], v[108:109], v[118:119], v[32:63]\n\t" \
  "ds_read_b64 v[108:109], v122 offset:16128\n\t" \
  "v_smfmac_f32_32x32x32_bf16 v[64:79], v[100:103], v[8:15], v120\n\t" \
  "v_smfmac_f32_32x32x32_bf16 v[80:95], v[100:103], v[24:31], v120\n\t" \
  "ds_read_b128 v[100:103], v123 offset:11456\n\t" \
  "s_add_u32 m0, %[w3k], 73728\n\t" \
  "s_nop 0\n\t" \
  "global_load_lds_dwordx4 v127, s[42:43] sc1\n\t" \
  "global_load_lds_dwordx4 v127, s[42:43] offset:1024 sc1\n\t" \
  "global_load_lds_dwordx4 v127, s[42:43] offset:2048 sc1\n\t" \
  "s_add_u32 s42, s42, 24576\n\t" \
  "s_addc_u32 s43, s43, 0\n\t" \
  "global_store_dwordx4 v201, v[176:179], s[40:41] sc1\n\t" \
  "global_store_dwordx4 v202, v[180:183], s[40:41] sc1\n\t" \
  "s_waitcnt vmcnt(14)\n\t" \
  "s_barrier\n\t" \
  "s_setprio 0\n\t" \
  "v_exp_f32 v32, v32\n\t" \
  "v_exp_f32 v33, v33\n\t" \
  "v_exp_f32 v34, v34\n\t" \
  "v_exp_f32 v35, v35\n\t" \
  "v_exp_f32 v36, v36\n\t" \
  "v_exp_f32 v37, v37\n\t" \
  "v_exp_f32 v38, v38\n\t" \
  "v_exp_f32 v39, v39\n\t" \
  "v_exp_f32 v48, v48\n\t" \
  "v_exp_f32 v49, v49\n\t" \
  "v_exp_f32 v50, v50\n\t" \
  "v_exp_f32 v51, v51\n\t" \
  "v_exp_f32 v52, v52\n\t" \
  "v_exp_f32 v53, v53\n\t" \
  "v_exp_f32 v54, v54\n\t" \
  "v_exp_f32 v55, v55\n\t" \
  "v_exp_f32 v40, v40\n\t" \
  "v_exp_f32 v41, v41\n\t" \
  "v_exp_f32 v42, v42\n\t" \
  "v_exp_f32 v43, v43\n\t" \
  "v_exp_f32 v44, v44\n\t" \
  "v_exp_f32 v45, v45\n\t" \
  "v_exp_f32 v46, v46\n\t" \
  "v_exp_f32 v47, v47\n\t" \
  "s_setprio 1\n\t" \
  "v_exp_f32 v56, v56\n\t" \
  "v_exp_f32 v57, v57\n\t" \
  "v_exp_f32 v58, v58\n\t" \
  "v_exp_f32 v59, v59\n\t" \
  "v_exp_f32 v60, v60\n\t" \
  "v_exp_f32 v61, v61\n\t" \
  "v_exp_f32 v62, v62\n\t" \
  "v_exp_f32 v63, v63\n\t" \
  "s_setprio 3\n\t" \
  "s_waitcnt lgkmcnt(0)\n\t" \
  "v_mfma_f32_32x32x16_bf16 v[0:15], v[104:107], v[110:113], 0\n\t" \
  "v_mfma_f32_32x32x16_bf16 v[16:31], v[104:107], v[114:117], 0\n\t" \
  "v_add_u32 v121, 21504, v121\n\t" \
  "ds_read_b128 v[104:107], v121\n\t" \
  "v_smfmac_f32_32x32x32_bf16 v[64:79], v[96:99], v[32:39], v120\n\t" \
  "v_smfmac_f32_32x32x32_bf16 v[80:95], v[96:99], v[48:55], v120\n\t" \
  "ds_read_b128 v[96:99], v123 offset:16128\n\t" \
  "v_mfma_f32_32x32x4_2b_bf16 v[0:31], v[108:109], v[118:119], v[0:31]\n\t" \
  "v_add_u32 v122, 21504, v122\n\t" \
  "ds_read_b64 v[108:109], v122\n\t" \
  "v_smfmac_f32_32x32x32_bf16 v[64:79], v[100:103], v[40:47], v120\n\t" \
  "v_smfmac_f32_32x32x32_bf16 v[80:95], v[100:103], v[56:63], v120\n\t" \
  "ds_read_b128 v[100:103], v123 offset:16832\n\t" \
  "s_add_u32 m0, %[w3k], 98304\n\t" \
  "s_nop 0\n\t" \
  "global_load_lds_dwordx4 v127, s[42:43] sc1\n\t" \
  "global_load_lds_dwordx4 v127, s[42:43] offset:1024 sc1\n\t" \
  "global_load_lds_dwordx4 v127, s[42:43] offset:2048 sc1\n\t" \
  "s_add_u32 s42, s42, 24576\n\t" \
  "s_addc_u32 s43, s43, 0\n\t" \
  "s_waitcnt vmcnt(3)\n\t" \
  "s_barrier\n\t" \
  "s_setprio 0\n\t" \
  "v_exp_f32 v0, v0\n\t" \
  "v_exp_f32 v1, v1\n\t" \
  "v_exp_f32 v2, v2\n\t" \
  "v_exp_f32 v3, v3\n\t" \
  "v_exp_f32 v4, v4\n\t" \
  "v_exp_f32 v5, v5\n\t" \
  "v_exp_f32 v6, v6\n\t" \
  "v_exp_f32 v7, v7\n\t" \
  "v_exp_f32 v16, v16\n\t" \
  "v_exp_f32 v17, v17\n\t" \
  "v_exp_f32 v18, v18\n\t" \
  "v_exp_f32 v19, v19\n\t" \
  "v_exp_f32 v20, v20\n\t" \
  "v_exp_f32 v21, v21\n\t" \
  "v_exp_f32 v22, v22\n\t" \
  "v_exp_f32 v23, v23\n\t" \
  "v_exp_f32 v8, v8\n\t" \
  "v_exp_f32 v9, v9\n\t" \
  "v_exp_f32 v10, v10\n\t" \
  "v_exp_f32 v11, v11\n\t" \
  "v_exp_f32 v12, v12\n\t" \
  "v_exp_f32 v13, v13\n\t" \
  "v_exp_f32 v14, v14\n\t" \
  "v_exp_f32 v15, v15\n\t" \
  "s_setprio 1\n\t" \
  "v_exp_f32 v24, v24\n\t" \
  "v_exp_f32 v25, v25\n\t" \
  "v_exp_f32 v26, v26\n\t" \
  "v_exp_f32 v27, v27\n\t" \
  "v_exp_f32 v28, v28\n\t" \
  "v_exp_f32 v29, v29\n\t" \
  "v_exp_f32 v30, v30\n\t" \
  "v_exp_f32 v31, v31\n\t" \
  "s_setprio 3\n\t" \
  "s_waitcnt lgkmcnt(0)\n\t" \
  "v_mfma_f32_32x32x16_bf16 v[32:47], v[104:107], v[110:113], 0\n\t" \
  "v_mfma_f32_32x32x16_bf16 v[48:63], v[104:107], v[114:117], 0\n\t" \
  "ds_read_b128 v[104:107], v121 offset:5376\n\t" \
  "v_smfmac_f32_32x32x32_bf16 v[64:79], v[96:99], v[0:7], v120\n\t" \
  "v_smfmac_f32_32x32x32_bf16 v[80:95], v[96:99], v[16:23], v120\n\t" \
  "v_add_u32 v123, 21504, v123\n\t" \
  "ds_read_b128 v[96:99], v123\n\t" \
  "v_mfma_f32_32x32x4_2b_bf16 v[32:63], v[108:109], v[118:119], v[32:63]\n\t" \
  "ds_read_b64 v[108:109], v122 offset:5376\n\t" \
  "v_smfmac_f32_32x32x32_bf16 v[64:79], v[100:103], v[8:15], v120\n\t" \
  "v_smfmac_f32_32x32x32_bf16 v[80:95], v[100:103], v[24:31], v120\n\t" \
  "ds_read_b128 v[100:103], v123 offset:704\n\t" \
  "s_add_u32 m0, %[w3k], 122880\n\t" \
  "s_nop 0\n\t" \
  "global_load_lds_dwordx4 v127, s[42:43] sc1\n\t" \
  "global_load_lds_dwordx4 v127, s[42:43] offset:1024 sc1\n\t" \
  "global_load_lds_dwordx4 v127, s[42:43] offset:2048 sc1\n\t" \
  "s_add_u32 s42, s42, 24576\n\t" \
  "s_addc_u32 s43, s43, 0\n\t" \
  "s_cmp_lg_u32 %[w3k], 0\n\t" \
  "s_cbranch_scc1 La301_%=\n\t" \
  "s_add_u32 m0, %[w3k], 147456\n\t" \
  "s_nop 0\n\t" \
  "global_load_lds_dwordx4 v127, s[42:43] sc1\n\t" \
  "global_load_lds_dwordx4 v127, s[42:43] offset:1024 sc1\n\t" \
  "global_load_lds_dwordx4 v127, s[42:43] offset:2048 sc1\n\t" \
  "La301_%=:\n\t" \
  "s_setprio 0\n\t" \
  "v_exp_f32 v32, v32\n\t" \
  "v_exp_f32 v33, v33\n\t" \
  "v_exp_f32 v34, v34\n\t" \
  "v_exp_f32 v35, v35\n\t" \
  "v_exp_f32 v36, v36\n\t" \
  "v_exp_f32 v37, v37\n\t" \
  "v_exp_f32 v38, v38\n\t" \
  "v_exp_f32 v39, v39\n\t" \
  "v_exp_f32 v48, v48\n\t" \
  "v_exp_f32 v49, v49\n\t" \
  "v_exp_f32 v50, v50\n\t" \
  "v_exp_f32 v51, v51\n\t" \
  "v_exp_f32 v52, v52\n\t" \
  "v_exp_f32 v53, v53\n\t" \
  "v_exp_f32 v54, v54\n\t" \
  "v_exp_f32 v55, v55\n\t" \
  "v_exp_f32 v40, v40\n\t" \
  "v_exp_f32 v41, v41\n\t" \
  "v_exp_f32 v42, v42\n\t" \
  "v_exp_f32 v43, v43\n\t" \
  "v_exp_f32 v44, v44\n\t" \
  "v_exp_f32 v45, v45\n\t" \
  "v_exp_f32 v46, v46\n\t" \
  "v_exp_f32 v47, v47\n\t" \
  "s_setprio 1\n\t" \
  "v_exp_f32 v56, v56\n\t" \
  "v_exp_f32 v57, v57\n\t" \
  "v_exp_f32 v58, v58\n\t" \
  "v_exp_f32 v59, v59\n\t" \
  "v_exp_f32 v60, v60\n\t" \
  "v_exp_f32 v61, v61\n\t" \
  "v_exp_f32 v62, v62\n\t" \
  "v_exp_f32 v63, v63\n\t" \
  "s_setprio 3\n\t" \
  "s_waitcnt lgkmcnt(0)\n\t" \
  "v_mfma_f32_32x32x16_bf16 v[0:15], v[104:107], v[110:113], 0\n\t" \
  "v_mfma_f32_32x32x16_bf16 v[16:31], v[104:107], v[114:117], 0\n\t" \
  "ds_read_b128 v[104:107], v121 offset:10752\n\t" \
  "v_smfmac_f32_32x32x32_bf16 v[64:79], v[96:99], v[32:39], v120\n\t" \
  "v_smfmac_f32_32x32x32_bf16 v[80:95], v[96:99], v[48:55], v120\n\t" \
  "ds_read_b128 v[96:99], v123 offset:5376\n\t" \
  "v_mfma_f32_32x32x4_2b_bf16 v[0:31], v[108:109], v[118:119], v[0:31]\n\t" \
  "ds_read_b64 v[108:109], v122 offset:10752\n\t" \
  "v_smfmac_f32_32x32x32_bf16 v[64:79], v[100:103], v[40:47], v120\n\t" \
  "v_smfmac_f32_32x32x32_bf16 v[80:95], v[100:103], v[56:63], v120\n\t" \
  "ds_read_b128 v[100:103], v123 offset:6080\n\t" \
  "s_setprio 0\n\t" \
  "v_exp_f32 v0, v0\n\t" \
  "v_exp_f32 v1, v1\n\t" \
  "v_exp_f32 v2, v2\n\t" \
  "v_exp_f32 v3, v3\n\t" \
  "v_exp_f32 v4, v4\n\t" \
  "v_exp_f32 v5, v5\n\t" \
  "v_exp_f32 v6, v6\n\t" \
  "v_exp_f32 v7, v7\n\t" \
  "v_exp_f32 v16, v16\n\t" \
  "v_exp_f32 v17, v17\n\t" \
  "v_exp_f32 v18, v18\n\t" \
  "v_exp_f32 v19, v19\n\t" \
  "v_exp_f32 v20, v20\n\t" \
  "v_exp_f32 v21, v21\n\t" \
  "v_exp_f32 v22, v22\n\t" \
  "v_exp_f32 v23, v23\n\t" \
  "v_exp_f32 v8, v8\n\t" \
  "v_exp_f32 v9, v9\n\t" \
  "v_exp_f32 v10, v10\n\t" \
  "v_exp_f32 v11, v11\n\t" \
  "v_exp_f32 v12, v12\n\t" \
  "v_exp_f32 v13, v13\n\t" \
  "v_exp_f32 v14, v14\n\t" \
  "v_exp_f32 v15, v15\n\t" \
  "s_setprio 1\n\t" \
  "v_exp_f32 v24, v24\n\t" \
  "v_exp_f32 v25, v25\n\t" \
  "v_exp_f32 v26, v26\n\t" \
  "v_exp_f32 v27, v27\n\t" \
  "v_exp_f32 v28, v28\n\t" \
  "v_exp_f32 v29, v29\n\t" \
  "v_exp_f32 v30, v30\n\t" \
  "v_exp_f32 v31, v31\n\t" \
  "s_setprio 3\n\t" \
  "s_waitcnt lgkmcnt(0)\n\t" \
  "v_mfma_f32_32x32x16_bf16 v[32:47], v[104:107], v[110:113], 0\n\t" \
  "v_mfma_f32_32x32x16_bf16 v[48:63], v[104:107], v[114:117], 0\n\t" \
  "ds_read_b128 v[104:107], v121 offset:16128\n\t" \
  "v_smfmac_f32_32x32x32_bf16 v[64:79], v[96:99], v[0:7], v120\n\t" \
  "v_smfmac_f32_32x32x32_bf16 v[80:95], v[96:99], v[16:23], v120\n\t" \
  "ds_read_b128 v[96:99], v123 offset:10752\n\t" \
  "v_mfma_f32_32x32x4_2b_bf16 v[32:63], v[108:109], v[118:119], v[32:63]\n\t" \
  "ds_read_b64 v[108:109], v122 offset:16128\n\t" \
  "v_smfmac_f32_32x32x32_bf16 v[64:79], v[100:103], v[8:15], v120\n\t" \
  "v_smfmac_f32_32x32x32_bf16 v[80:95], v[100:103], v[24:31], v120\n\t" \
  "ds_read_b128 v[100:103], v123 offset:11456\n\t" \
  "s_waitcnt vmcnt(0)\n\t" \
  "s_barrier\n\t" \
  "s_add_u32 s42, s42, 3072\n\t" \
  "s_addc_u32 s43, s43, 0\n\t" \
  "s_add_u32 m0, %[w3k], 0\n\t" \
  "s_nop 0\n\t" \
  "global_load_lds_dwordx4 v127, s[42:43] sc1\n\t" \
  "global_load_lds_dwordx4 v127, s[42:43] offset:1024 sc1\n\t" \
  "global_load_lds_dwordx4 v127, s[42:43] offset:2048 sc1\n\t" \
  "s_setprio 0\n\t" \
  "v_exp_f32 v32, v32\n\t" \
  "v_exp_f32 v33, v33\n\t" \
  "v_exp_f32 v34, v34\n\t" \
  "v_exp_f32 v35, v35\n\t" \
  "v_exp_f32 v36, v36\n\t" \
  "v_exp_f32 v37, v37\n\t" \
  "v_exp_f32 v38, v38\n\t" \
  "v_exp_f32 v39, v39\n\t" \
  "v_exp_f32 v48, v48\n\t" \
  "v_exp_f32 v49, v49\n\t" \
  "v_exp_f32 v50, v50\n\t" \
  "v_exp_f32 v51, v51\n\t" \
  "v_exp_f32 v52, v52\n\t" \
  "v_exp_f32 v53, v53\n\t" \
  "v_exp_f32 v54, v54\n\t" \
  "v_exp_f32 v55, v55\n\t" \
  "v_exp_f32 v40, v40\n\t" \
  "v_exp_f32 v41, v41\n\t" \
  "v_exp_f32 v42, v42\n\t" \
  "v_exp_f32 v43, v43\n\t" \
  "v_exp_f32 v44, v44\n\t" \
  "v_exp_f32 v45, v45\n\t" \
  "v_exp_f32 v46, v46\n\t" \
  "v_exp_f32 v47, v47\n\t" \
  "s_setprio 1\n\t" \
  "v_exp_f32 v56, v56\n\t" \
  "v_exp_f32 v57, v57\n\t" \
  "v_exp_f32 v58, v58\n\t" \
  "v_exp_f32 v59, v59\n\t" \
  "v_exp_f32 v60, v60\n\t" \
  "v_exp_f32 v61, v61\n\t" \
  "v_exp_f32 v62, v62\n\t" \
  "v_exp_f32 v63, v63\n\t" \
  "s_setprio 3\n\t" \
  "s_waitcnt lgkmcnt(0)\n\t" \
  "v_mfma_f32_32x32x16_bf16 v[0:15], v[104:107], v[110:113], 0\n\t" \
  "v_mfma_f32_32x32x16_bf16 v[16:31], v[104:107], v[114:117], 0\n\t" \
  "v_add_u32 v121, 21504, v121\n\t" \
  "ds_read_b128 v[104:107], v121\n\t" \
  "v_smfmac_f32_32x32x32_bf16 v[64:79], v[96:99], v[32:39], v120\n\t" \
  "v_smfmac_f32_32x32x32_bf16 v[80:95], v[96:99], v[48:55], v120\n\t" \
  "ds_read_b128 v[96:99], v123 offset:16128\n\t" \
  "v_mfma_f32_32x32x4_2b_bf16 v[0:31], v[108:109], v[118:119], v[0:31]\n\t" \
  "v_add_u32 v122, 21504, v122\n\t" \
  "ds_read_b64 v[108:109], v122\n\t" \
  "v_smfmac_f32_32x32x32_bf16 v[64:79], v[100:103], v[40:47], v120\n\t" \
  "v_smfmac_f32_32x32x32_bf16 v[80:95], v[100:103], v[56:63], v120\n\t" \
  "ds_read_b128 v[100:103], v123 offset:16832\n\t" \
  "s_setprio 0\n\t" \
  "v_exp_f32 v0, v0\n\t" \
  "v_exp_f32 v1, v1\n\t" \
  "v_exp_f32 v2, v2\n\t" \
  "v_exp_f32 v3, v3\n\t" \
  "v_exp_f32 v4, v4\n\t" \
  "v_exp_f32 v5, v5\n\t" \
  "v_exp_f32 v6, v6\n\t" \
  "v_exp_f32 v7, v7\n\t" \
  "v_exp_f32 v16, v16\n\t" \
  "v_exp_f32 v17, v17\n\t" \
  "v_exp_f32 v18, v18\n\t" \
  "v_exp_f32 v19, v19\n\t" \
  "v_exp_f32 v20, v20\n\t" \
  "v_exp_f32 v21, v21\n\t" \
  "v_exp_f32 v22, v22\n\t" \
  "v_exp_f32 v23, v23\n\t" \
  "v_exp_f32 v8, v8\n\t" \
  "v_exp_f32 v9, v9\n\t" \
  "v_exp_f32 v10, v10\n\t" \
  "v_exp_f32 v11, v11\n\t" \
  "v_exp_f32 v12, v12\n\t" \
  "v_exp_f32 v13, v13\n\t" \
  "v_exp_f32 v14, v14\n\t" \
  "v_exp_f32 v15, v15\n\t" \
  "s_setprio 1\n\t" \
  "v_exp_f32 v24, v24\n\t" \
  "v_exp_f32 v25, v25\n\t" \
  "v_exp_f32 v26, v26\n\t" \
  "v_exp_f32 v27, v27\n\t" \
  "v_exp_f32 v28, v28\n\t" \
  "v_exp_f32 v29, v29\n\t" \
  "v_exp_f32 v30, v30\n\t" \
  "v_exp_f32 v31, v31\n\t" \
  "s_setprio 3\n\t" \
  "s_waitcnt lgkmcnt(0)\n\t" \
  "v_mfma_f32_32x32x16_bf16 v[32:47], v[104:107], v[110:113], 0\n\t" \
  "v_mfma_f32_32x32x16_bf16 v[48:63], v[104:107], v[114:117], 0\n\t" \
  "ds_read_b128 v[104:107], v121 offset:5376\n\t" \
  "v_smfmac_f32_32x32x32_bf16 v[64:79], v[96:99], v[0:7], v120\n\t" \
  "v_smfmac_f32_32x32x32_bf16 v[80:95], v[96:99], v[16:23], v120\n\t" \
  "v_add_u32 v123, 21504, v123\n\t" \
  "ds_read_b128 v[96:99], v123\n\t" \
  "v_mfma_f32_32x32x4_2b_bf16 v[32:63], v[108:109], v[118:119], v[32:63]\n\t" \
  "ds_read_b64 v[108:109], v122 offset:5376\n\t" \
  "v_smfmac_f32_32x32x32_bf16 v[64:79], v[100:103], v[8:15], v120\n\t" \
  "v_smfmac_f32_32x32x32_bf16 v[80:95], v[100:103], v[24:31], v120\n\t" \
  "ds_read_b128 v[100:103], v123 offset:704\n\t" \
  "s_barrier\n\t" \
  "s_mov_b32 s46, 2\n\t" \
  "s_mov_b32 s49, 0\n\t" \
  "loopa302_%=:\n\t" \
  "s_cmp_eq_u32 s46, 6\n\t" \
  "s_cselect_b32 s47, -129024, %[slot]\n\t" \
  "s_setprio 0\n\t" \
  "v_exp_f32 v32, v32\n\t" \
  "v_exp_f32 v33, v33\n\t" \
  "v_exp_f32 v34, v34\n\t" \
  "v_exp_f32 v35, v35\n\t" \
  "v_exp_f32 v36, v36\n\t" \
  "v_exp_f32 v37, v37\n\t" \
  "v_exp_f32 v38, v38\n\t" \
  "v_exp_f32 v39, v39\n\t" \
  "v_exp_f32 v48, v48\n\t" \
  "v_exp_f32 v49, v49\n\t" \
  "v_exp_f32 v50, v50\n\t" \
  "v_exp_f32 v51, v51\n\t" \
  "v_exp_f32 v52, v52\n\t" \
  "v_exp_f32 v53, v53\n\t" \
  "v_exp_f32 v54, v54\n\t" \
  "v_exp_f32 v55, v55\n\t" \
  "v_exp_f32 v40, v40\n\t" \
  "v_exp_f32 v41, v41\n\t" \
  "v_exp_f32 v42, v42\n\t" \
  "v_exp_f32 v43, v43\n\t" \
  "v_exp_f32 v44, v44\n\t" \
  "v_exp_f32 v45, v45\n\t" \
  "v_exp_f32 v46, v46\n\t" \
  "v_exp_f32 v47, v47\n\t" \
  "s_setprio 1\n\t" \
  "v_exp_f32 v56, v56\n\t" \
  "v_exp_f32 v57, v57\n\t" \
  "v_exp_f32 v58, v58\n\t" \
  "v_exp_f32 v59, v59\n\t" \
  "v_exp_f32 v60, v60\n\t" \
  "v_exp_f32 v61, v61\n\t" \
  "v_exp_f32 v62, v62\n\t" \
  "v_exp_f32 v63, v63\n\t" \
  "s_setprio 3\n\t" \
  "s_waitcnt lgkmcnt(0)\n\t" \
  "v_mfma_f32_32x32x16_bf16 v[0:15], v[104:107], v[110:113], 0\n\t" \
  "v_mfma_f32_32x32x16_bf16 v[16:31], v[104:107], v[114:117], 0\n\t" \
  "ds_read_b128 v[104:107], v121 offset:10752\n\t" \
  "v_smfmac_f32_32x32x32_bf16 v[64:79], v[96:99], v[32:39], v120\n\t" \
  "v_smfmac_f32_32x32x32_bf16 v[80:95], v[96:99], v[48:55], v120\n\t" \
  "ds_read_b128 v[96:99], v123 offset:5376\n\t" \
  "v_mfma_f32_32x32x4_2b_bf16 v[0:31], v[108:109], v[118:119], v[0:31]\n\t" \
  "ds_read_b64 v[108:109], v122 offset:10752\n\t" \
  "v_smfmac_f32_32x32x32_bf16 v[64:79], v[100:103], v[40:47], v120\n\t" \
  "v_smfmac_f32_32x32x32_bf16 v[80:95], v[100:103], v[56:63], v120\n\t" \
  "ds_read_b128 v[100:103], v123 offset:6080\n\t" \
  "s_cmp_eq_u32 s46, 2\n\t" \
  "s_cbranch_scc1 La303_%=\n\t" \
  "s_waitcnt vmcnt(4)\n\t" \
  "v_mfma_f32_32x32x16_bf16 v[128:143], v[180:183], v[176:179], v[128:143]\n\t" \
  "v_mfma_f32_32x32x16_bf16 v[144:159], v[184:187], v[176:179], v[144:159]\n\t" \
  "v_mfma_f32_32x32x16_bf16 v[160:175], v[188:191], v[176:179], v[160:175]\n\t" \
  "s_branch La304_%=\n\t" \
  "La303_%=:\n\t" \
  "s_mov_b64 s[52:53], s[44:45]\n\t" \
  "global_load_dwordx4 v[216:219], v211, s[52:53] sc1\n\t" \
  "global_load_dwordx4 v[220:223], v211, s[52:53] offset:1024 sc1\n\t" \
  "global_load_dwordx4 v[224:227], v211, s[52:53] offset:2048 sc1\n\t" \
  "global_load_dwordx4 v[228:231], v211, s[52:53] offset:3072 sc1\n\t" \
  "s_add_u32 s52, s52, 4096\n\t" \
  "s_addc_u32 s53, s53, 0\n\t" \
  "global_load_dwordx4 v[232:235], v211, s[52:53] sc1\n\t" \
  "La304_%=:\n\t" \
  "global_load_dwordx4 v[176:179], v208, s[40:41] sc1\n\t" \
  "global_load_dwordx4 v[180:183], v209, s[40:41] sc1\n\t" \
  "global_load_dwordx4 v[184:187], v210, s[40:41] sc1\n\t" \
  "global_load_dwordx4 v[188:191], v211, s[40:41] sc1\n\t" \
  "s_cmp_eq_u32 s49, 0\n\t" \
  "s_cbranch_scc1 La305_%=\n\t" \
  "s_mov_b32 m0, s50\n\t" \
  "s_sub_u32 s49, s49, 1\n\t" \
  "global_load_lds_dwordx4 v209, s[44:45] sc1\n\t" \
  "global_load_lds_dwordx4 v209, s[44:45] offset:1024 sc1\n\t" \
  "s_add_u32 s44, s44, 2048\n\t" \
  "s_addc_u32 s45, s45, 0\n\t" \
  "s_add_u32 s50, s50, 2048\n\t" \
  "La305_%=:\n\t" \
  "s_cmp_lg_u32 s46, 6\n\t" \
  "s_cbranch_scc1 La306_%=\n\t" \
  "s_barrier\n\t" \
  "s_mov_b32 s49, 5\n\t" \
  "La306_%=:\n\t" \
  "s_setprio 0\n\t" \
  "v_exp_f32 v0, v0\n\t" \
  "v_exp_f32 v1, v1\n\t" \
  "v_exp_f32 v2, v2\n\t" \
  "v_exp_f32 v3, v3\n\t" \
  "v_exp_f32 v4, v4\n\t" \
  "v_exp_f32 v5, v5\n\t" \
  "v_exp_f32 v6, v6\n\t" \
  "v_exp_f32 v7, v7\n\t" \
  "v_exp_f32 v16, v16\n\t" \
  "v_exp_f32 v17, v17\n\t" \
  "v_exp_f32 v18, v18\n\t" \
  "v_exp_f32 v19, v19\n\t" \
  "v_exp_f32 v20, v20\n\t" \
  "v_exp_f32 v21, v21\n\t" \
  "v_exp_f32 v22, v22\n\t" \
  "v_exp_f32 v23, v23\n\t" \
  "v_exp_f32 v8, v8\n\t" \
  "v_exp_f32 v9, v9\n\t" \
  "v_exp_f32 v10, v10\n\t" \
  "v_exp_f32 v11, v11\n\t" \
  "v_exp_f32 v12, v12\n\t" \
  "v_exp_f32 v13, v13\n\t" \
  "v_exp_f32 v14, v14\n\t" \
  "v_exp_f32 v15, v15\n\t" \
  "s_setprio 1\n\t" \
  "v_exp_f32 v24, v24\n\t" \
  "v_exp_f32 v25, v25\n\t" \
  "v_exp_f32 v26, v26\n\t" \
  "v_exp_f32 v27, v27\n\t" \
  "v_exp_f32 v28, v28\n\t" \
  "v_exp_f32 v29, v29\n\t" \
  "v_exp_f32 v30, v30\n\t" \
  "v_exp_f32 v31, v31\n\t" \
  "s_setprio 3\n\t" \
  "s_waitcnt lgkmcnt(0)\n\t" \
  "v_mfma_f32_32x32x16_bf16 v[32:47], v[104:107], v[110:113], 0\n\t" \
  "v_mfma_f32_32x32x16_bf16 v[48:63], v[104:107], v[114:117], 0\n\t" \
  "ds_read_b128 v[104:107], v121 offset:16128\n\t" \
  "v_smfmac_f32_32x32x32_bf16 v[64:79], v[96:99], v[0:7], v120\n\t" \
  "v_smfmac_f32_32x32x32_bf16 v[80:95], v[96:99], v[16:23], v120\n\t" \
  "ds_read_b128 v[96:99], v123 offset:10752\n\t" \
  "v_mfma_f32_32x32x4_2b_bf16 v[32:63], v[108:109], v[118:119], v[32:63]\n\t" \
  "ds_read_b64 v[108:109], v122 offset:16128\n\t" \
  "v_smfmac_f32_32x32x32_bf16 v[64:79], v[100:103], v[8:15], v120\n\t" \
  "v_smfmac_f32_32x32x32_bf16 v[80:95], v[100:103], v[24:31], v120\n\t" \
  "ds_read_b128 v[100:103], v123 offset:11456\n\t" \
  "s_cmp_eq_u32 s46, 2\n\t" \
  "s_cbranch_scc1 La307_%=\n\t" \
  "s_waitcnt vmcnt(4)\n\t" \
  "v_mfma_f32_32x32x16_bf16 v[128:143], v[196:199], v[192:195], v[128:143]\n\t" \
  "v_mfma_f32_32x32x16_bf16 v[144:159], v[200:203], v[192:195], v[144:159]\n\t" \
  "v_mfma_f32_32x32x16_bf16 v[160:175], v[204:207], v[192:195], v[160:175]\n\t" \
  "s_branch La308_%=\n\t" \
  "La307_%=:\n\t" \
  "global_load_dwordx4 v[236:239], v211, s[52:53] offset:1024 sc1\n\t" \
  "global_load_dwordx4 v[240:243], v211, s[52:53] offset:2048 sc1\n\t" \
  "global_load_dwordx4 v[244:247], v211, s[52:53] offset:3072 sc1\n\t" \
  "s_add_u32 s52, s52, 4096\n\t" \
  "s_addc_u32 s53, s53, 0\n\t" \
  "global_load_dwordx4 v[248:251], v211, s[52:53] sc1\n\t" \
  "global_load_dwordx4 v[252:255], v211, s[52:53] offset:1024 sc1\n\t" \
  "La308_%=:\n\t" \
  "global_load_dwordx4 v[192:195], v208, s[40:41] offset:1024 sc1\n\t" \
  "global_load_dwordx4 v[196:199], v209, s[40:41] offset:1024 sc1\n\t" \
  "global_load_dwordx4 v[200:203], v210, s[40:41] offset:1024 sc1\n\t" \
  "global_load_dwordx4 v[204:207], v211, s[40:41] offset:1024 sc1\n\t" \
  "s_cmp_eq_u32 s49, 0\n\t" \
  "s_cbranch_scc1 La309_%=\n\t" \
  "s_mov_b32 m0, s50\n\t" \
  "s_sub_u32 s49, s49, 1\n\t" \
  "global_load_lds_dwordx4 v209, s[44:45] sc1\n\t" \
  "global_load_lds_dwordx4 v209, s[44:45] offset:1024 sc1\n\t" \
  "s_add_u32 s44, s44, 2048\n\t" \
  "s_addc_u32 s45, s45, 0\n\t" \
  "s_add_u32 s50, s50, 2048\n\t" \
  "La309_%=:\n\t" \
  "s_setprio 0\n\t" \
  "v_exp_f32 v32, v32\n\t" \
  "v_exp_f32 v33, v33\n\t" \
  "v_exp_f32 v34, v34\n\t" \
  "v_exp_f32 v35, v35\n\t" \
  "v_exp_f32 v36, v36\n\t" \
  "v_exp_f32 v37, v37\n\t" \
  "v_exp_f32 v38, v38\n\t" \
  "v_exp_f32 v39, v39\n\t" \
  "v_exp_f32 v48, v48\n\t" \
  "v_exp_f32 v49, v49\n\t" \
  "v_exp_f32 v50, v50\n\t" \
  "v_exp_f32 v51, v51\n\t" \
  "v_exp_f32 v52, v52\n\t" \
  "v_exp_f32 v53, v53\n\t" \
  "v_exp_f32 v54, v54\n\t" \
  "v_exp_f32 v55, v55\n\t" \
  "v_exp_f32 v40, v40\n\t" \
  "v_exp_f32 v41, v41\n\t" \
  "v_exp_f32 v42, v42\n\t" \
  "v_exp_f32 v43, v43\n\t" \
  "v_exp_f32 v44, v44\n\t" \
  "v_exp_f32 v45, v45\n\t" \
  "v_exp_f32 v46, v46\n\t" \
  "v_exp_f32 v47, v47\n\t" \
  "s_setprio 1\n\t" \
  "v_exp_f32 v56, v56\n\t" \
  "v_exp_f32 v57, v57\n\t" \
  "v_exp_f32 v58, v58\n\t" \
  "v_exp_f32 v59, v59\n\t" \
  "v_exp_f32 v60, v60\n\t" \
  "v_exp_f32 v61, v61\n\t" \
  "v_exp_f32 v62, v62\n\t" \
  "v_exp_f32 v63, v63\n\t" \
  "s_setprio 3\n\t" \
  "s_waitcnt lgkmcnt(0)\n\t" \
  "v_mfma_f32_32x32x16_bf16 v[0:15], v[104:107], v[110:113], 0\n\t" \
  "v_mfma_f32_32x32x16_bf16 v[16:31], v[104:107], v[114:117], 0\n\t" \
  "v_add_u32 v121, s47, v121\n\t" \
  "ds_read_b128 v[104:107], v121\n\t" \
  "v_smfmac_f32_32x32x32_bf16 v[64:79], v[96:99], v[32:39], v120\n\t" \
  "v_smfmac_f32_32x32x32_bf16 v[80:95], v[96:99], v[48:55], v120\n\t" \
  "ds_read_b128 v[96:99], v123 offset:16128\n\t" \
  "v_mfma_f32_32x32x4_2b_bf16 v[0:31], v[108:109], v[118:119], v[0:31]\n\t" \
  "v_add_u32 v122, s47, v122\n\t" \
  "ds_read_b64 v[108:109], v122\n\t" \
  "v_smfmac_f32_32x32x32_bf16 v[64:79], v[100:103], v[40:47], v120\n\t" \
  "v_smfmac_f32_32x32x32_bf16 v[80:95], v[100:103], v[56:63], v120\n\t" \
  "ds_read_b128 v[100:103], v123 offset:16832\n\t" \
  "s_waitcnt vmcnt(4)\n\t" \
  "v_mfma_f32_32x32x16_bf16 v[128:143], v[180:183], v[176:179], v[128:143]\n\t" \
  "v_mfma_f32_32x32x16_bf16 v[144:159], v[184:187], v[176:179], v[144:159]\n\t" \
  "v_mfma_f32_32x32x16_bf16 v[160:175], v[188:191], v[176:179], v[160:175]\n\t" \
  "global_load_dwordx4 v[176:179], v208, s[40:41] offset:2048 sc1\n\t" \
  "global_load_dwordx4 v[180:183], v209, s[40:41] offset:2048 sc1\n\t" \
  "global_load_dwordx4 v[184:187], v210, s[40:41] offset:2048 sc1\n\t" \
  "global_load_dwordx4 v[188:191], v211, s[40:41] offset:2048 sc1\n\t" \
  "s_cmp_eq_u32 s49, 0\n\t" \
  "s_cbranch_scc1 La3010_%=\n\t" \
  "s_mov_b32 m0, s50\n\t" \
  "s_sub_u32 s49, s49, 1\n\t" \
  "global_load_lds_dwordx4 v209, s[44:45] sc1\n\t" \
  "global_load_lds_dwordx4 v209, s[44:45] offset:1024 sc1\n\t" \
  "s_add_u32 s44, s44, 2048\n\t" \
  "s_addc_u32 s45, s45, 0\n\t" \
  "s_add_u32 s50, s50, 2048\n\t" \
  "La3010_%=:\n\t" \
  "s_setprio 0\n\t" \
  "v_exp_f32 v0, v0\n\t" \
  "v_exp_f32 v1, v1\n\t" \
  "v_exp_f32 v2, v2\n\t" \
  "v_exp_f32 v3, v3\n\t" \
  "v_exp_f32 v4, v4\n\t" \
  "v_exp_f32 v5, v5\n\t" \
  "v_exp_f32 v6, v6\n\t" \
  "v_exp_f32 v7, v7\n\t" \
  "v_exp_f32 v16, v16\n\t" \
  "v_exp_f32 v17, v17\n\t" \
  "v_exp_f32 v18, v18\n\t" \
  "v_exp_f32 v19, v19\n\t" \
  "v_exp_f32 v20, v20\n\t" \
  "v_exp_f32 v21, v21\n\t" \
  "v_exp_f32 v22, v22\n\t" \
  "v_exp_f32 v23, v23\n\t" \
  "v_exp_f32 v8, v8\n\t" \
  "v_exp_f32 v9, v9\n\t" \
  "v_exp_f32 v10, v10\n\t" \
  "v_exp_f32 v11, v11\n\t" \
  "v_exp_f32 v12, v12\n\t" \
  "v_exp_f32 v13, v13\n\t" \
  "v_exp_f32 v14, v14\n\t" \
  "v_exp_f32 v15, v15\n\t" \
  "s_setprio 1\n\t" \
  "v_exp_f32 v24, v24\n\t" \
  "v_exp_f32 v25, v25\n\t" \
  "v_exp_f32 v26, v26\n\t" \
  "v_exp_f32 v27, v27\n\t" \
  "v_exp_f32 v28, v28\n\t" \
  "v_exp_f32 v29, v29\n\t" \
  "v_exp_f32 v30, v30\n\t" \
  "v_exp_f32 v31, v31\n\t" \
  "s_setprio 3\n\t" \
  "s_waitcnt lgkmcnt(0)\n\t" \
  "v_mfma_f32_32x32x16_bf16 v[32:47], v[104:107], v[110:113], 0\n\t" \
  "v_mfma_f32_32x32x16_bf16 v[48:63], v[104:107], v[114:117], 0\n\t" \
  "ds_read_b128 v[104:107], v121 offset:5376\n\t" \
  "v_smfmac_f32_32x32x32_bf16 v[64:79], v[96:99], v[0:7], v120\n\t" \
  "v_smfmac_f32_32x32x32_bf16 v[80:95], v[96:99], v[16:23], v120\n\t" \
  "v_add_u32 v123, s47, v123\n\t" \
  "ds_read_b128 v[96:99], v123\n\t" \
  "v_mfma_f32_32x32x4_2b_bf16 v[32:63], v[108:109], v[118:119], v[32:63]\n\t" \
  "ds_read_b64 v[108:109], v122 offset:5376\n\t" \
  "v_smfmac_f32_32x32x32_bf16 v[64:79], v[100:103], v[8:15], v120\n\t" \
  "v_smfmac_f32_32x32x32_bf16 v[80:95], v[100:103], v[24:31], v120\n\t" \
  "ds_read_b128 v[100:103], v123 offset:704\n\t" \
  "s_waitcnt vmcnt(4)\n\t" \
  "v_mfma_f32_32x32x16_bf16 v[128:143], v[196:199], v[192:195], v[128:143]\n\t" \
  "v_mfma_f32_32x32x16_bf16 v[144:159], v[200:203], v[192:195], v[144:159]\n\t" \
  "v_mfma_f32_32x32x16_bf16 v[160:175], v[204:207], v[192:195], v[160:175]\n\t" \
  "global_load_dwordx4 v[192:195], v208, s[40:41] offset:3072 sc1\n\t" \
  "global_load_dwordx4 v[196:199], v209, s[40:41] offset:3072 sc1\n\t" \
  "global_load_dwordx4 v[200:203], v210, s[40:41] offset:3072 sc1\n\t" \
  "global_load_dwordx4 v[204:207], v211, s[40:41] offset:3072 sc1\n\t" \
  "s_add_u32 s40, s40, 4096\n\t" \
  "s_addc_u32 s41, s41, 0\n\t" \
  "s_cmp_eq_u32 s49, 0\n\t" \
  "s_cbranch_scc1 La3011_%=\n\t" \
  "s_mov_b32 m0, s50\n\t" \
  "s_sub_u32 s49, s49, 1\n\t" \
  "global_load_lds_dwordx4 v209, s[44:45] sc1\n\t" \
  "global_load_lds_dwordx4 v209, s[44:45] offset:1024 sc1\n\t" \
  "s_add_u32 s44, s44, 2048\n\t" \
  "s_addc_u32 s45, s45, 0\n\t" \
  "s_add_u32 s50, s50, 2048\n\t" \
  "La3011_%=:\n\t" \
  "s_add_u32 s46, s46, 1\n\t" \
  "s_cmp_lt_u32 s46, 8\n\t" \
  "s_cbranch_scc1 loopa302_%=\n\t" \
  "s_nop 7\n\t" \
  "s_nop 3\n\t" \
  "ds_write_b128 v124, v[80:83]\n\t" \
  "ds_write_b128 v124, v[84:87] offset:1024\n\t" \
  "ds_write_b128 v124, v[88:91] offset:2048\n\t" \
  "s_waitcnt vmcnt(8) lgkmcnt(0)\n\t" \
  "s_barrier\n\t" \
  "ds_read_b128 v[0:3], v125\n\t" \
  "ds_read_b128 v[4:7], v125 offset:1024\n\t" \
  "ds_read_b128 v[8:11], v125 offset:2048\n\t" \
  "s_waitcnt lgkmcnt(2)\n\t" \
  "v_add_f32 v0, v0, v64\n\t" \
  "v_add_f32 v1, v1, v65\n\t" \
  "v_add_f32 v2, v2, v66\n\t" \
  "v_add_f32 v3, v3, v67\n\t" \
  "s_waitcnt lgkmcnt(1)\n\t" \
  "v_add_f32 v4, v4, v68\n\t" \
  "v_add_f32 v5, v5, v69\n\t" \
  "v_add_f32 v6, v6, v70\n\t" \
  "v_add_f32 v7, v7, v71\n\t" \
  "s_waitcnt lgkmcnt(0)\n\t" \
  "v_add_f32 v8, v8, v72\n\t" \
  "v_add_f32 v9, v9, v73\n\t" \
  "v_add_f32 v10, v10, v74\n\t" \
  "v_add_f32 v11, v11, v75\n\t" \
  "s_setprio 0\n\t" \
  "s_branch Ea3_%=\n\t" \
  "Sa30_%=:\n\t" \
  "global_load_dword v212, v203, s[40:41] sc1\n\t" \
  "global_load_dword v213, v204, s[40:41] sc1\n\t" \
  "v_mov_b64 v[64:65], 0\n\t" \
  "v_mov_b64 v[66:67], 0\n\t" \
  "v_mov_b64 v[68:69], 0\n\t" \
  "v_mov_b64 v[70:71], 0\n\t" \
  "v_mov_b64 v[72:73], 0\n\t" \
  "v_mov_b64 v[74:75], 0\n\t" \
  "v_mov_b64 v[76:77], 0\n\t" \
  "v_mov_b64 v[78:79], 0\n\t" \
  "v_mov_b64 v[80:81], 0\n\t" \
  "v_mov_b64 v[82:83], 0\n\t" \
  "v_mov_b64 v[84:85], 0\n\t" \
  "v_mov_b64 v[86:87], 0\n\t" \
  "v_mov_b64 v[88:89], 0\n\t" \
  "v_mov_b64 v[90:91], 0\n\t" \
  "v_mov_b64 v[92:93], 0\n\t" \
  "v_mov_b64 v[94:95], 0\n\t" \
  "s_waitcnt vmcnt(5)\n\t" \
  "s_setprio 0\n\t" \
  "s_barrier\n\t" \
  "ds_read_b128 v[104:107], v121\n\t" \
  "ds_read_b64 v[108:109], v122\n\t" \
  "ds_read_b128 v[96:99], v123\n\t" \
  "ds_read_b128 v[100:103], v123 offset:704\n\t" \
  "s_waitcnt lgkmcnt(3)\n\t" \
  "v_mfma_f32_32x32x16_bf16 v[32:47], v[104:107], v[110:113], 0\n\t" \
  "v_mfma_f32_32x32x16_bf16 v[48:63], v[104:107], v[114:117], 0\n\t" \
  "ds_read_b128 v[104:107], v121 offset:5376\n\t" \
  "s_waitcnt lgkmcnt(3)\n\t" \
  "v_mul_f32 v0, v0, |v0|\n\t" \
  "v_mul_f32 v1, v1, |v1|\n\t" \
  "v_cvt_pk_bf16_f32 v216, v0, v1\n\t" \
  "v_pk_max_i16 v216, v216, 0\n\t" \
  "v_mul_f32 v2, v2, |v2|\n\t" \
  "v_mul_f32 v3, v3, |v3|\n\t" \
  "v_cvt_pk_bf16_f32 v217, v2, v3\n\t" \
  "v_pk_max_i16 v217, v217, 0\n\t" \
  "v_mul_f32 v4, v4, |v4|\n\t" \
  "v_mul_f32 v5, v5, |v5|\n\t" \
  "v_cvt_pk_bf16_f32 v218, v4, v5\n\t" \
  "v_pk_max_i16 v218, v218, 0\n\t" \
  "v_mfma_f32_32x32x4_2b_bf16 v[32:63], v[108:109], v[118:119], v[32:63]\n\t" \
  "ds_read_b64 v[108:109], v122 offset:5376\n\t" \
  "v_mul_f32 v6, v6, |v6|\n\t" \
  "v_mul_f32 v7, v7, |v7|\n\t" \
  "v_cvt_pk_bf16_f32 v219, v6, v7\n\t" \
  "v_pk_max_i16 v219, v219, 0\n\t" \
  "v_mul_f32 v8, v8, |v8|\n\t" \
  "v_mul_f32 v9, v9, |v9|\n\t" \
  "v_cvt_pk_bf16_f32 v220, v8, v9\n\t" \
  "v_pk_max_i16 v220, v220, 0\n\t" \
  "v_mul_f32 v10, v10, |v10|\n\t" \
  "v_mul_f32 v11, v11, |v11|\n\t" \
  "v_cvt_pk_bf16_f32 v221, v10, v11\n\t" \
  "v_pk_max_i16 v221, v221, 0\n\t" \
  "v_mul_f32 v12, v12, |v12|\n\t" \
  "v_mul_f32 v13, v13, |v13|\n\t" \
  "v_cvt_pk_bf16_f32 v222, v12, v13\n\t" \
  "v_pk_max_i16 v222, v222, 0\n\t" \
  "v_mul_f32 v14, v14, |v14|\n\t" \
  "v_mul_f32 v15, v15, |v15|\n\t" \
  "v_cvt_pk_bf16_f32 v223, v14, v15\n\t" \
  "v_pk_max_i16 v223, v223, 0\n\t" \
  "global_store_dwordx4 v200, v[216:219], s[40:41] sc1\n\t" \
  "global_store_dwordx4 v200, v[220:223], s[40:41] offset:1024 sc1\n\t" \
  "s_setprio 0\n\t" \
  "v_exp_f32 v32, v32\n\t" \
  "v_exp_f32 v33, v33\n\t" \
  "v_exp_f32 v34, v34\n\t" \
  "v_exp_f32 v35, v35\n\t" \
  "v_exp_f32 v36, v36\n\t" \
  "v_exp_f32 v37, v37\n\t" \
  "v_exp_f32 v38, v38\n\t" \
  "v_exp_f32 v39, v39\n\t" \
  "v_exp_f32 v48, v48\n\t" \
  "v_exp_f32 v49, v49\n\t" \
  "v_exp_f32 v50, v50\n\t" \
  "v_exp_f32 v51, v51\n\t" \
  "v_exp_f32 v52, v52\n\t" \
  "v_exp_f32 v53, v53\n\t" \
  "v_exp_f32 v54, v54\n\t" \
  "v_exp_f32 v55, v55\n\t" \
  "v_exp_f32 v40, v40\n\t" \
  "v_exp_f32 v41, v41\n\t" \
  "v_exp_f32 v42, v42\n\t" \
  "v_exp_f32 v43, v43\n\t" \
  "v_exp_f32 v44, v44\n\t" \
  "v_exp_f32 v45, v45\n\t" \
  "v_exp_f32 v46, v46\n\t" \
  "v_exp_f32 v47, v47\n\t" \
  "s_setprio 1\n\t" \
  "v_exp_f32 v56, v56\n\t" \
  "v_exp_f32 v57, v57\n\t" \
  "v_exp_f32 v58, v58\n\t" \
  "v_exp_f32 v59, v59\n\t" \
  "v_exp_f32 v60, v60\n\t" \
  "v_exp_f32 v61, v61\n\t" \
  "v_exp_f32 v62, v62\n\t" \
  "v_exp_f32 v63, v63\n\t" \
  "s_setprio 3\n\t" \
  "s_waitcnt lgkmcnt(0)\n\t" \
  "v_mfma_f32_32x32x16_bf16 v[0:15], v[104:107], v[110:113], 0\n\t" \
  "v_mfma_f32_32x32x16_bf16 v[16:31], v[104:107], v[114:117], 0\n\t" \
  "ds_read_b128 v[104:107], v121 offset:10752\n\t" \
  "v_smfmac_f32_32x32x32_bf16 v[64:79], v[96:99], v[32:39], v120\n\t" \
  "v_smfmac_f32_32x32x32_bf16 v[80:95], v[96:99], v[48:55], v120\n\t" \
  "ds_read_b128 v[96:99], v123 offset:5376\n\t" \
  "v_mfma_f32_32x32x4_2b_bf16 v[0:31], v[108:109], v[118:119], v[0:31]\n\t" \
  "ds_read_b64 v[108:109], v122 offset:10752\n\t" \
  "v_smfmac_f32_32x32x32_bf16 v[64:79], v[100:103], v[40:47], v120\n\t" \
  "v_smfmac_f32_32x32x32_bf16 v[80:95], v[100:103], v[56:63], v120\n\t" \
  "ds_read_b128 v[100:103], v123 offset:6080\n\t" \
  "s_add_u32 m0, %[w3k], 49152\n\t" \
  "s_nop 0\n\t" \
  "global_load_lds_dwordx4 v127, s[42:43] sc1\n\t" \
  "global_load_lds_dwordx4 v127, s[42:43] offset:1024 sc1\n\t" \
  "global_load_lds_dwordx4 v127, s[42:43] offset:2048 sc1\n\t" \
  "s_add_u32 s42, s42, 24576\n\t" \
  "s_addc_u32 s43, s43, 0\n\t" \
  "global_store_dwordx4 v201, v[176:179], s[40:41] sc1\n\t" \
  "global_store_dwordx4 v202, v[180:183], s[40:41] sc1\n\t" \
  "s_setprio 0\n\t" \
  "v_exp_f32 v0, v0\n\t" \
  "v_exp_f32 v1, v1\n\t" \
  "v_exp_f32 v2, v2\n\t" \
  "v_exp_f32 v3, v3\n\t" \
  "v_exp_f32 v4, v4\n\t" \
  "v_exp_f32 v5, v5\n\t" \
  "v_exp_f32 v6, v6\n\t" \
  "v_exp_f32 v7, v7\n\t" \
  "v_exp_f32 v16, v16\n\t" \
  "v_exp_f32 v17, v17\n\t" \
  "v_exp_f32 v18, v18\n\t" \
  "v_exp_f32 v19, v19\n\t" \
  "v_exp_f32 v20, v20\n\t" \
  "v_exp_f32 v21, v21\n\t" \
  "v_exp_f32 v22, v22\n\t" \
  "v_exp_f32 v23, v23\n\t" \
  "v_exp_f32 v8, v8\n\t" \
  "v_exp_f32 v9, v9\n\t" \
  "v_exp_f32 v10, v10\n\t" \
  "v_exp_f32 v11, v11\n\t" \
  "v_exp_f32 v12, v12\n\t" \
  "v_exp_f32 v13, v13\n\t" \
  "v_exp_f32 v14, v14\n\t" \
  "v_exp_f32 v15, v15\n\t" \
  "s_setprio 1\n\t" \
  "v_exp_f32 v24, v24\n\t" \
  "v_exp_f32 v25, v25\n\t" \
  "v_exp_f32 v26, v26\n\t" \
  "v_exp_f32 v27, v27\n\t" \
  "v_exp_f32 v28, v28\n\t" \
  "v_exp_f32 v29, v29\n\t" \
  "v_exp_f32 v30, v30\n\t" \
  "v_exp_f32 v31, v31\n\t" \
  "s_setprio 3\n\t" \
  "s_waitcnt lgkmcnt(0)\n\t" \
  "s_waitcnt vmcnt(9)\n\t" \
  "s_barrier\n\t" \
  "v_mfma_f32_32x32x16_bf16 v[32:47], v[104:107], v[110:113], 0\n\t" \
  "v_mfma_f32_32x32x16_bf16 v[48:63], v[104:107], v[114:117], 0\n\t" \
  "ds_read_b128 v[104:107], v121 offset:16128\n\t" \
  "v_smfmac_f32_32x32x32_bf16 v[64:79], v[96:99], v[0:7], v120\n\t" \
  "v_smfmac_f32_32x32x32_bf16 v[80:95], v[96:99], v[16:23], v120\n\t" \
  "ds_read_b128 v[96:99], v123 offset:10752\n\t" \
  "v_mfma_f32_32x32x4_2b_bf16 v[32:63], v[108:109], v[118:119], v[32:63]\n\t" \
  "ds_read_b64 v[108:109], v122 offset:16128\n\t" \
  "v_smfmac_f32_32x32x32_bf16 v[64:79], v[100:103], v[8:15], v120\n\t" \
  "v_smfmac_f32_32x32x32_bf16 v[80:95], v[100:103], v[24:31], v120\n\t" \
  "ds_read_b128 v[100:103], v123 offset:11456\n\t" \
  "s_add_u32 m0, %[w3k], 73728\n\t" \
  "s_nop 0\n\t" \
  "global_load_lds_dwordx4 v127, s[42:43] sc1\n\t" \
  "global_load_lds_dwordx4 v127, s[42:43] offset:1024 sc1\n\t" \
  "global_load_lds_dwordx4 v127, s[42:43] offset:2048 sc1\n\t" \
  "s_add_u32 s42, s42, 24576\n\t" \
  "s_addc_u32 s43, s43, 0\n\t" \
  "s_setprio 0\n\t" \
  "v_exp_f32 v32, v32\n\t" \
  "v_exp_f32 v33, v33\n\t" \
  "v_exp_f32 v34, v34\n\t" \
  "v_exp_f32 v35, v35\n\t" \
  "v_exp_f32 v36, v36\n\t" \
  "v_exp_f32 v37, v37\n\t" \
  "v_exp_f32 v38, v38\n\t" \
  "v_exp_f32 v39, v39\n\t" \
  "v_exp_f32 v48, v48\n\t" \
  "v_exp_f32 v49, v49\n\t" \
  "v_exp_f32 v50, v50\n\t" \
  "v_exp_f32 v51, v51\n\t" \
  "v_exp_f32 v52, v52\n\t" \
  "v_exp_f32 v53, v53\n\t" \
  "v_exp_f32 v54, v54\n\t" \
  "v_exp_f32 v55, v55\n\t" \
  "v_exp_f32 v40, v40\n\t" \
  "v_exp_f32 v41, v41\n\t" \
  "v_exp_f32 v42, v42\n\t" \
  "v_exp_f32 v43, v43\n\t" \
  "v_exp_f32 v44, v44\n\t" \
  "v_exp_f32 v45, v45\n\t" \
  "v_exp_f32 v46, v46\n\t" \
  "v_exp_f32 v47, v47\n\t" \
  "s_setprio 1\n\t" \
  "v_exp_f32 v56, v56\n\t" \
  "v_exp_f32 v57, v57\n\t" \
  "v_exp_f32 v58, v58\n\t" \
  "v_exp_f32 v59, v59\n\t" \
  "v_exp_f32 v60, v60\n\t" \
  "v_exp_f32 v61, v61\n\t" \
  "v_exp_f32 v62, v62\n\t" \
  "v_exp_f32 v63, v63\n\t" \
  "s_setprio 3\n\t" \
  "s_waitcnt lgkmcnt(0)\n\t" \
  "s_waitcnt vmcnt(3)\n\t" \
  "s_barrier\n\t" \
  "s_bitcmp1_b32 %[fl], 0\n\t" \
  "s_cbranch_scc0 La311_%=\n\t" \
  "v_mov_b32 v214, 1\n\t" \
  "s_mov_b64 exec, 0xffff\n\t" \
  "global_store_dword v205, v214, %[fb] sc1\n\t" \
  "s_mov_b64 exec, -1\n\t" \
  "La311_%=:\n\t" \
  "v_mfma_f32_32x32x16_bf16 v[0:15], v[104:107], v[110:113], 0\n\t" \
  "v_mfma_f32_32x32x16_bf16 v[16:31], v[104:107], v[114:117], 0\n\t" \
  "v_add_u32 v121, 21504, v121\n\t" \
  "ds_read_b128 v[104:107], v121\n\t" \
  "v_smfmac_f32_32x32x32_bf16 v[64:79], v[96:99], v[32:39], v120\n\t" \
  "v_smfmac_f32_32x32x32_bf16 v[80:95], v[96:99], v[48:55], v120\n\t" \
  "ds_read_b128 v[96:99], v123 offset:16128\n\t" \
  "v_mfma_f32_32x32x4_2b_bf16 v[0:31], v[108:109], v[118:119], v[0:31]\n\t" \
  "v_add_u32 v122, 21504, v122\n\t" \
  "ds_read_b64 v[108:109], v122\n\t" \
  "v_smfmac_f32_32x32x32_bf16 v[64:79], v[100:103], v[40:47], v120\n\t" \
  "v_smfmac_f32_32x32x32_bf16 v[80:95], v[100:103], v[56:63], v120\n\t" \
  "ds_read_b128 v[100:103], v123 offset:16832\n\t" \
  "s_add_u32 m0, %[w3k], 98304\n\t" \
  "s_nop 0\n\t" \
  "global_load_lds_dwordx4 v127, s[42:43] sc1\n\t" \
  "global_load_lds_dwordx4 v127, s[42:43] offset:1024 sc1\n\t" \
  "global_load_lds_dwordx4 v127, s[42:43] offset:2048 sc1\n\t" \
  "s_add_u32 s42, s42, 24576\n\t" \
  "s_addc_u32 s43, s43, 0\n\t" \
  "s_setprio 0\n\t" \
  "v_exp_f32 v0, v0\n\t" \
  "v_exp_f32 v1, v1\n\t" \
  "v_exp_f32 v2, v2\n\t" \
  "v_exp_f32 v3, v3\n\t" \
  "v_exp_f32 v4, v4\n\t" \
  "v_exp_f32 v5, v5\n\t" \
  "v_exp_f32 v6, v6\n\t" \
  "v_exp_f32 v7, v7\n\t" \
  "v_exp_f32 v16, v16\n\t" \
  "v_exp_f32 v17, v17\n\t" \
  "v_exp_f32 v18, v18\n\t" \
  "v_exp_f32 v19, v19\n\t" \
  "v_exp_f32 v20, v20\n\t" \
  "v_exp_f32 v21, v21\n\t" \
  "v_exp_f32 v22, v22\n\t" \
  "v_exp_f32 v23, v23\n\t" \
  "v_exp_f32 v8, v8\n\t" \
  "v_exp_f32 v9, v9\n\t" \
  "v_exp_f32 v10, v10\n\t" \
  "v_exp_f32 v11, v11\n\t" \
  "v_exp_f32 v12, v12\n\t" \
  "v_exp_f32 v13, v13\n\t" \
  "v_exp_f32 v14, v14\n\t" \
  "v_exp_f32 v15, v15\n\t" \
  "s_setprio 1\n\t" \
  "v_exp_f32 v24, v24\n\t" \
  "v_exp_f32 v25, v25\n\t" \
  "v_exp_f32 v26, v26\n\t" \
  "v_exp_f32 v27, v27\n\t" \
  "v_exp_f32 v28, v28\n\t" \
  "v_exp_f32 v29, v29\n\t" \
  "v_exp_f32 v30, v30\n\t" \
  "v_exp_f32 v31, v31\n\t" \
  "s_setprio 3\n\t" \
  "s_waitcnt lgkmcnt(0)\n\t" \
  "v_mfma_f32_32x32x16_bf16 v[32:47], v[104:107], v[110:113], 0\n\t" \
  "v_mfma_f32_32x32x16_bf16 v[48:63], v[104:107], v[114:117], 0\n\t" \
  "ds_read_b128 v[104:107], v121 offset:5376\n\t" \
  "v_smfmac_f32_32x32x32_bf16 v[64:79], v[96:99], v[0:7], v120\n\t" \
  "v_smfmac_f32_32x32x32_bf16 v[80:95], v[96:99], v[16:23], v120\n\t" \
  "v_add_u32 v123, 21504, v123\n\t" \
  "ds_read_b128 v[96:99], v123\n\t" \
  "v_mfma_f32_32x32x4_2b_bf16 v[32:63], v[108:109], v[118:119], v[32:63]\n\t" \
  "ds_read_b64 v[108:109], v122 offset:5376\n\t" \
  "v_smfmac_f32_32x32x32_bf16 v[64:79], v[100:103], v[8:15], v120\n\t" \
  "v_smfmac_f32_32x32x32_bf16 v[80:95], v[100:103], v[24:31], v120\n\t" \
  "ds_read_b128 v[100:103], v123 offset:704\n\t" \
  "s_add_u32 m0, %[w3k], 122880\n\t" \
  "s_nop 0\n\t" \
  "global_load_lds_dwordx4 v127, s[42:43] sc1\n\t" \
  "global_load_lds_dwordx4 v127, s[42:43] offset:1024 sc1\n\t" \
  "global_load_lds_dwordx4 v127, s[42:43] offset:2048 sc1\n\t" \
  "s_add_u32 s42, s42, 24576\n\t" \
  "s_addc_u32 s43, s43, 0\n\t" \
  "s_setprio 0\n\t" \
  "v_exp_f32 v32, v32\n\t" \
  "v_exp_f32 v33, v33\n\t" \
  "v_exp_f32 v34, v34\n\t" \
  "v_exp_f32 v35, v35\n\t" \
  "v_exp_f32 v36, v36\n\t" \
  "v_exp_f32 v37, v37\n\t" \
  "v_exp_f32 v38, v38\n\t" \
  "v_exp_f32 v39, v39\n\t" \
  "v_exp_f32 v48, v48\n\t" \
  "v_exp_f32 v49, v49\n\t" \
  "v_exp_f32 v50, v50\n\t" \
  "v_exp_f32 v51, v51\n\t" \
  "v_exp_f32 v52, v52\n\t" \
  "v_exp_f32 v53, v53\n\t" \
  "v_exp_f32 v54, v54\n\t" \
  "v_exp_f32 v55, v55\n\t" \
  "v_exp_f32 v40, v40\n\t" \
  "v_exp_f32 v41, v41\n\t" \
  "v_exp_f32 v42, v42\n\t" \
  "v_exp_f32 v43, v43\n\t" \
  "v_exp_f32 v44, v44\n\t" \
  "v_exp_f32 v45, v45\n\t" \
  "v_exp_f32 v46, v46\n\t" \
  "v_exp_f32 v47, v47\n\t" \
  "s_setprio 1\n\t" \
  "v_exp_f32 v56, v56\n\t" \
  "v_exp_f32 v57, v57\n\t" \
  "v_exp_f32 v58, v58\n\t" \
  "v_exp_f32 v59, v59\n\t" \
  "v_exp_f32 v60, v60\n\t" \
  "v_exp_f32 v61, v61\n\t" \
  "v_exp_f32 v62, v62\n\t" \
  "v_exp_f32 v63, v63\n\t" \
  "s_setprio 3\n\t" \
  "s_waitcnt lgkmcnt(0)\n\t" \
  "v_mfma_f32_32x32x16_bf16 v[0:15], v[104:107], v[110:113], 0\n\t" \
  "v_mfma_f32_32x32x16_bf16 v[16:31], v[104:107], v[114:117], 0\n\t" \
  "ds_read_b128 v[104:107], v121 offset:10752\n\t" \
  "v_smfmac_f32_32x32x32_bf16 v[64:79], v[96:99], v[32:39], v120\n\t" \
  "v_smfmac_f32_32x32x32_bf16 v[80:95], v[96:99], v[48:55], v120\n\t" \
  "ds_read_b128 v[96:99], v123 offset:5376\n\t" \
  "v_mfma_f32_32x32x4_2b_bf16 v[0:31], v[108:109], v[118:119], v[0:31]\n\t" \
  "ds_read_b64 v[108:109], v122 offset:10752\n\t" \
  "v_smfmac_f32_32x32x32_bf16 v[64:79], v[100:103], v[40:47], v120\n\t" \
  "v_smfmac_f32_32x32x32_bf16 v[80:95], v[100:103], v[56:63], v120\n\t" \
  "ds_read_b128 v[100:103], v123 offset:6080\n\t" \
  "s_setprio 0\n\t" \
  "v_exp_f32 v0, v0\n\t" \
  "v_exp_f32 v1, v1\n\t" \
  "v_exp_f32 v2, v2\n\t" \
  "v_exp_f32 v3, v3\n\t" \
  "v_exp_f32 v4, v4\n\t" \
  "v_exp_f32 v5, v5\n\t" \
  "v_exp_f32 v6, v6\n\t" \
  "v_exp_f32 v7, v7\n\t" \
  "v_exp_f32 v16, v16\n\t" \
  "v_exp_f32 v17, v17\n\t" \
  "v_exp_f32 v18, v18\n\t" \
  "v_exp_f32 v19, v19\n\t" \
  "v_exp_f32 v20, v20\n\t" \
  "v_exp_f32 v21, v21\n\t" \
  "v_exp_f32 v22, v22\n\t" \
  "v_exp_f32 v23, v23\n\t" \
  "v_exp_f32 v8, v8\n\t" \
  "v_exp_f32 v9, v9\n\t" \
  "v_exp_f32 v10, v10\n\t" \
  "v_exp_f32 v11, v11\n\t" \
  "v_exp_f32 v12, v12\n\t" \
  "v_exp_f32 v13, v13\n\t" \
  "v_exp_f32 v14, v14\n\t" \
  "v_exp_f32 v15, v15\n\t" \
  "s_setprio 1\n\t" \
  "v_exp_f32 v24, v24\n\t" \
  "v_exp_f32 v25, v25\n\t" \
  "v_exp_f32 v26, v26\n\t" \
  "v_exp_f32 v27, v27\n\t" \
  "v_exp_f32 v28, v28\n\t" \
  "v_exp_f32 v29, v29\n\t" \
  "v_exp_f32 v30, v30\n\t" \
  "v_exp_f32 v31, v31\n\t" \
  "s_setprio 3\n\t" \
  "s_waitcnt lgkmcnt(0)\n\t" \
  "s_waitcnt vmcnt(0)\n\t" \
  "s_barrier\n\t" \
  "v_mfma_f32_32x32x16_bf16 v[32:47], v[104:107], v[110:113], 0\n\t" \
  "v_mfma_f32_32x32x16_bf16 v[48:63], v[104:107], v[114:117], 0\n\t" \
  "ds_read_b128 v[104:107], v121 offset:16128\n\t" \
  "v_smfmac_f32_32x32x32_bf16 v[64:79], v[96:99], v[0:7], v120\n\t" \
  "v_smfmac_f32_32x32x32_bf16 v[80:95], v[96:99], v[16:23], v120\n\t" \
  "ds_read_b128 v[96:99], v123 offset:10752\n\t" \
  "v_mfma_f32_32x32x4_2b_bf16 v[32:63], v[108:109], v[118:119], v[32:63]\n\t" \
  "ds_read_b64 v[108:109], v122 offset:16128\n\t" \
  "v_smfmac_f32_32x32x32_bf16 v[64:79], v[100:103], v[8:15], v120\n\t" \
  "v_smfmac_f32_32x32x32_bf16 v[80:95], v[100:103], v[24:31], v120\n\t" \
  "ds_read_b128 v[100:103], v123 offset:11456\n\t" \
  "s_bitcmp1_b32 %[fl], 0\n\t" \
  "s_cbranch_scc1 La312_%=\n\t" \
  "s_add_u32 s42, s42, 3072\n\t" \
  "s_addc_u32 s43, s43, 0\n\t" \
  "s_add_u32 m0, %[w3k], 0\n\t" \
  "s_nop 0\n\t" \
  "global_load_lds_dwordx4 v127, s[42:43] sc1\n\t" \
  "global_load_lds_dwordx4 v127, s[42:43] offset:1024 sc1\n\t" \
  "global_load_lds_dwordx4 v127, s[42:43] offset:2048 sc1\n\t" \
  "La312_%=:\n\t" \
  "s_bitcmp1_b32 %[fl], 0\n\t" \
  "s_cbranch_scc0 La313_%=\n\t" \
  "v_add_u32 v126, 16384, v126\n\t" \
  "global_load_dwordx4 v[184:187], v126, %[fb] sc1\n\t" \
  "La313_%=:\n\t" \
  "s_setprio 0\n\t" \
  "v_exp_f32 v32, v32\n\t" \
  "v_exp_f32 v33, v33\n\t" \
  "v_exp_f32 v34, v34\n\t" \
  "v_exp_f32 v35, v35\n\t" \
  "v_exp_f32 v36, v36\n\t" \
  "v_exp_f32 v37, v37\n\t" \
  "v_exp_f32 v38, v38\n\t" \
  "v_exp_f32 v39, v39\n\t" \
  "v_exp_f32 v48, v48\n\t" \
  "v_exp_f32 v49, v49\n\t" \
  "v_exp_f32 v50, v50\n\t" \
  "v_exp_f32 v51, v51\n\t" \
  "v_exp_f32 v52, v52\n\t" \
  "v_exp_f32 v53, v53\n\t" \
  "v_exp_f32 v54, v54\n\t" \
  "v_exp_f32 v55, v55\n\t" \
  "v_exp_f32 v40, v40\n\t" \
  "v_exp_f32 v41, v41\n\t" \
  "v_exp_f32 v42, v42\n\t" \
  "v_exp_f32 v43, v43\n\t" \
  "v_exp_f32 v44, v44\n\t" \
  "v_exp_f32 v45, v45\n\t" \
  "v_exp_f32 v46, v46\n\t" \
  "v_exp_f32 v47, v47\n\t" \
  "s_setprio 1\n\t" \
  "v_exp_f32 v56, v56\n\t" \
  "v_exp_f32 v57, v57\n\t" \
  "v_exp_f32 v58, v58\n\t" \
  "v_exp_f32 v59, v59\n\t" \
  "v_exp_f32 v60, v60\n\t" \
  "v_exp_f32 v61, v61\n\t" \
  "v_exp_f32 v62, v62\n\t" \
  "v_exp_f32 v63, v63\n\t" \
  "s_setprio 3\n\t" \
  "s_waitcnt lgkmcnt(0)\n\t" \
  "v_mfma_f32_32x32x16_bf16 v[0:15], v[104:107], v[110:113], 0\n\t" \
  "v_mfma_f32_32x32x16_bf16 v[16:31], v[104:107], v[114:117], 0\n\t" \
  "v_add_u32 v121, 21504, v121\n\t" \
  "ds_read_b128 v[104:107], v121\n\t" \
  "v_smfmac_f32_32x32x32_bf16 v[64:79], v[96:99], v[32:39], v120\n\t" \
  "v_smfmac_f32_32x32x32_bf16 v[80:95], v[96:99], v[48:55], v120\n\t" \
  "ds_read_b128 v[96:99], v123 offset:16128\n\t" \
  "v_mfma_f32_32x32x4_2b_bf16 v[0:31], v[108:109], v[118:119], v[0:31]\n\t" \
  "v_add_u32 v122, 21504, v122\n\t" \
  "ds_read_b64 v[108:109], v122\n\t" \
  "v_smfmac_f32_32x32x32_bf16 v[64:79], v[100:103], v[40:47], v120\n\t" \
  "v_smfmac_f32_32x32x32_bf16 v[80:95], v[100:103], v[56:63], v120\n\t" \
  "ds_read_b128 v[100:103], v123 offset:16832\n\t" \
  "s_setprio 0\n\t" \
  "v_exp_f32 v0, v0\n\t" \
  "v_exp_f32 v1, v1\n\t" \
  "v_exp_f32 v2, v2\n\t" \
  "v_exp_f32 v3, v3\n\t" \
  "v_exp_f32 v4, v4\n\t" \
  "v_exp_f32 v5, v5\n\t" \
  "v_exp_f32 v6, v6\n\t" \
  "v_exp_f32 v7, v7\n\t" \
  "v_exp_f32 v16, v16\n\t" \
  "v_exp_f32 v17, v17\n\t" \
  "v_exp_f32 v18, v18\n\t" \
  "v_exp_f32 v19, v19\n\t" \
  "v_exp_f32 v20, v20\n\t" \
  "v_exp_f32 v21, v21\n\t" \
  "v_exp_f32 v22, v22\n\t" \
  "v_exp_f32 v23, v23\n\t" \
  "v_exp_f32 v8, v8\n\t" \
  "v_exp_f32 v9, v9\n\t" \
  "v_exp_f32 v10, v10\n\t" \
  "v_exp_f32 v11, v11\n\t" \
  "v_exp_f32 v12, v12\n\t" \
  "v_exp_f32 v13, v13\n\t" \
  "v_exp_f32 v14, v14\n\t" \
  "v_exp_f32 v15, v15\n\t" \
  "s_setprio 1\n\t" \
  "v_exp_f32 v24, v24\n\t" \
  "v_exp_f32 v25, v25\n\t" \
  "v_exp_f32 v26, v26\n\t" \
  "v_exp_f32 v27, v27\n\t" \
  "v_exp_f32 v28, v28\n\t" \
  "v_exp_f32 v29, v29\n\t" \
  "v_exp_f32 v30, v30\n\t" \
  "v_exp_f32 v31, v31\n\t" \
  "s_setprio 3\n\t" \
  "s_waitcnt lgkmcnt(0)\n\t" \
  "s_bitcmp1_b32 %[fl], 0\n\t" \
  "s_cbranch_scc0 La314_%=\n\t" \
  "s_mov_b32 s48, 0x100000\n\t" \
  "F2a316_%=:\n\t" \
  "s_waitcnt vmcnt(0)\n\t" \
  "v_and_b32 v184, v184, v185\n\t" \
  "v_and_b32 v186, v186, v187\n\t" \
  "v_and_b32 v184, v184, v186\n\t" \
  "v_cmp_ne_u32 vcc, 1, v184\n\t" \
  "s_cbranch_vccz F2a315_%=\n\t" \
  "global_load_dwordx4 v[184:187], v126, %[fb] sc1\n\t" \
  "s_sub_u32 s48, s48, 1\n\t" \
  "s_cbranch_scc0 F2a316_%=\n\t" \
  "s_waitcnt vmcnt(0)\n\t" \
  "F2a315_%=:\n\t" \
  "La314_%=:\n\t" \
  "s_barrier\n\t" \
  "v_mfma_f32_32x32x16_bf16 v[32:47], v[104:107], v[110:113], 0\n\t" \
  "v_mfma_f32_32x32x16_bf16 v[48:63], v[104:107], v[114:117], 0\n\t" \
  "ds_read_b128 v[104:107], v121 offset:5376\n\t" \
  "v_smfmac_f32_32x32x32_bf16 v[64:79], v[96:99], v[0:7], v120\n\t" \
  "v_smfmac_f32_32x32x32_bf16 v[80:95], v[96:99], v[16:23], v120\n\t" \
  "v_add_u32 v123, 21504, v123\n\t" \
  "ds_read_b128 v[96:99], v123\n\t" \
  "v_mfma_f32_32x32x4_2b_bf16 v[32:63], v[108:109], v[118:119], v[32:63]\n\t" \
  "ds_read_b64 v[108:109], v122 offset:5376\n\t" \
  "v_smfmac_f32_32x32x32_bf16 v[64:79], v[100:103], v[8:15], v120\n\t" \
  "v_smfmac_f32_32x32x32_bf16 v[80:95], v[100:103], v[24:31], v120\n\t" \
  "ds_read_b128 v[100:103], v123 offset:704\n\t" \
  "s_mov_b32 s46, 2\n\t" \
  "s_mov_b32 s49, 0\n\t" \
  "loopa317_%=:\n\t" \
  "s_cmp_eq_u32 s46, 6\n\t" \
  "s_cselect_b32 s47, -129024, %[slot]\n\t" \
  "s_setprio 0\n\t" \
  "v_exp_f32 v32, v32\n\t" \
  "v_exp_f32 v33, v33\n\t" \
  "v_exp_f32 v34, v34\n\t" \
  "v_exp_f32 v35, v35\n\t" \
  "v_exp_f32 v36, v36\n\t" \
  "v_exp_f32 v37, v37\n\t" \
  "v_exp_f32 v38, v38\n\t" \
  "v_exp_f32 v39, v39\n\t" \
  "v_exp_f32 v48, v48\n\t" \
  "v_exp_f32 v49, v49\n\t" \
  "v_exp_f32 v50, v50\n\t" \
  "v_exp_f32 v51, v51\n\t" \
  "v_exp_f32 v52, v52\n\t" \
  "v_exp_f32 v53, v53\n\t" \
  "v_exp_f32 v54, v54\n\t" \
  "v_exp_f32 v55, v55\n\t" \
  "v_exp_f32 v40, v40\n\t" \
  "v_exp_f32 v41, v41\n\t" \
  "v_exp_f32 v42, v42\n\t" \
  "v_exp_f32 v43, v43\n\t" \
  "v_exp_f32 v44, v44\n\t" \
  "v_exp_f32 v45, v45\n\t" \
  "v_exp_f32 v46, v46\n\t" \
  "v_exp_f32 v47, v47\n\t" \
  "s_setprio 1\n\t" \
  "v_exp_f32 v56, v56\n\t" \
  "v_exp_f32 v57, v57\n\t" \
  "v_exp_f32 v58, v58\n\t" \
  "v_exp_f32 v59, v59\n\t" \
  "v_exp_f32 v60, v60\n\t" \
  "v_exp_f32 v61, v61\n\t" \
  "v_exp_f32 v62, v62\n\t" \
  "v_exp_f32 v63, v63\n\t" \
  "s_setprio 3\n\t" \
  "s_waitcnt lgkmcnt(0)\n\t" \
  "s_cmp_lg_u32 s46, 6\n\t" \
  "s_cbranch_scc1 La318_%=\n\t" \
  "s_barrier\n\t" \
  "s_mov_b32 s49, 5\n\t" \
  "La318_%=:\n\t" \
  "v_mfma_f32_32x32x16_bf16 v[0:15], v[104:107], v[110:113], 0\n\t" \
  "v_mfma_f32_32x32x16_bf16 v[16:31], v[104:107], v[114:117], 0\n\t" \
  "ds_read_b128 v[104:107], v121 offset:10752\n\t" \
  "v_smfmac_f32_32x32x32_bf16 v[64:79], v[96:99], v[32:39], v120\n\t" \
  "v_smfmac_f32_32x32x32_bf16 v[80:95], v[96:99], v[48:55], v120\n\t" \
  "ds_read_b128 v[96:99], v123 offset:5376\n\t" \
  "v_mfma_f32_32x32x4_2b_bf16 v[0:31], v[108:109], v[118:119], v[0:31]\n\t" \
  "ds_read_b64 v[108:109], v122 offset:10752\n\t" \
  "v_smfmac_f32_32x32x32_bf16 v[64:79], v[100:103], v[40:47], v120\n\t" \
  "v_smfmac_f32_32x32x32_bf16 v[80:95], v[100:103], v[56:63], v120\n\t" \
  "ds_read_b128 v[100:103], v123 offset:6080\n\t" \
  "s_cmp_eq_u32 s46, 2\n\t" \
  "s_cbranch_scc1 La319_%=\n\t" \
  "s_waitcnt vmcnt(3)\n\t" \
  "v_mfma_f32_32x32x16_bf16 v[128:143], v[180:183], v[176:179], v[128:143]\n\t" \
  "v_mfma_f32_32x32x16_bf16 v[144:159], v[184:187], v[176:179], v[144:159]\n\t" \
  "La319_%=:\n\t" \
  "global_load_dwordx4 v[176:179], v208, s[40:41] sc1\n\t" \
  "global_load_dwordx4 v[180:183], v209, s[40:41] sc1\n\t" \
  "global_load_dwordx4 v[184:187], v210, s[40:41] sc1\n\t" \
  "s_cmp_eq_u32 s49, 0\n\t" \
  "s_cbranch_scc1 La3111_%=\n\t" \
  "s_mov_b32 m0, s50\n\t" \
  "s_sub_u32 s49, s49, 1\n\t" \
  "global_load_lds_dwordx4 v210, s[44:45] sc1\n\t" \
  "global_load_lds_dwordx4 v210, s[44:45] offset:1024 sc1\n\t" \
  "s_add_u32 s44, s44, 2048\n\t" \
  "s_addc_u32 s45, s45, 0\n\t" \
  "s_add_u32 s50, s50, 2048\n\t" \
  "La3111_%=:\n\t" \
  "s_setprio 0\n\t" \
  "v_exp_f32 v0, v0\n\t" \
  "v_exp_f32 v1, v1\n\t" \
  "v_exp_f32 v2, v2\n\t" \
  "v_exp_f32 v3, v3\n\t" \
  "v_exp_f32 v4, v4\n\t" \
  "v_exp_f32 v5, v5\n\t" \
  "v_exp_f32 v6, v6\n\t" \
  "v_exp_f32 v7, v7\n\t" \
  "v_exp_f32 v16, v16\n\t" \
  "v_exp_f32 v17, v17\n\t" \
  "v_exp_f32 v18, v18\n\t" \
  "v_exp_f32 v19, v19\n\t" \
  "v_exp_f32 v20, v20\n\t" \
  "v_exp_f32 v21, v21\n\t" \
  "v_exp_f32 v22, v22\n\t" \
  "v_exp_f32 v23, v23\n\t" \
  "v_exp_f32 v8, v8\n\t" \
  "v_exp_f32 v9, v9\n\t" \
  "v_exp_f32 v10, v10\n\t" \
  "v_exp_f32 v11, v11\n\t" \
  "v_exp_f32 v12, v12\n\t" \
  "v_exp_f32 v13, v13\n\t" \
  "v_exp_f32 v14, v14\n\t" \
  "v_exp_f32 v15, v15\n\t" \
  "s_setprio 1\n\t" \
  "v_exp_f32 v24, v24\n\t" \
  "v_exp_f32 v25, v25\n\t" \
  "v_exp_f32 v26, v26\n\t" \
  "v_exp_f32 v27, v27\n\t" \
  "v_exp_f32 v28, v28\n\t" \
  "v_exp_f32 v29, v29\n\t" \
  "v_exp_f32 v30, v30\n\t" \
  "v_exp_f32 v31, v31\n\t" \
  "s_setprio 3\n\t" \
  "s_waitcnt lgkmcnt(0)\n\t" \
  "v_mfma_f32_32x32x16_bf16 v[32:47], v[104:107], v[110:113], 0\n\t" \
  "v_mfma_f32_32x32x16_bf16 v[48:63], v[104:107], v[114:117], 0\n\t" \
  "ds_read_b128 v[104:107], v121 offset:16128\n\t" \
  "v_smfmac_f32_32x32x32_bf16 v[64:79], v[96:99], v[0:7], v120\n\t" \
  "v_smfmac_f32_32x32x32_bf16 v[80:95], v[96:99], v[16:23], v120\n\t" \
  "ds_read_b128 v[96:99], v123 offset:10752\n\t" \
  "v_mfma_f32_32x32x4_2b_bf16 v[32:63], v[108:109], v[118:119], v[32:63]\n\t" \
  "ds_read_b64 v[108:109], v122 offset:16128\n\t" \
  "v_smfmac_f32_32x32x32_bf16 v[64:79], v[100:103], v[8:15], v120\n\t" \
  "v_smfmac_f32_32x32x32_bf16 v[80:95], v[100:103], v[24:31], v120\n\t" \
  "ds_read_b128 v[100:103], v123 offset:11456\n\t" \
  "s_cmp_eq_u32 s46, 2\n\t" \
  "s_cbranch_scc1 La3112_%=\n\t" \
  "s_waitcnt vmcnt(3)\n\t" \
  "v_mfma_f32_32x32x16_bf16 v[128:143], v[196:199], v[192:195], v[128:143]\n\t" \
  "v_mfma_f32_32x32x16_bf16 v[144:159], v[200:203], v[192:195], v[144:159]\n\t" \
  "La3112_%=:\n\t" \
  "global_load_dwordx4 v[192:195], v208, s[40:41] offset:1024 sc1\n\t" \
  "global_load_dwordx4 v[196:199], v209, s[40:41] offset:1024 sc1\n\t" \
  "global_load_dwordx4 v[200:203], v210, s[40:41] offset:1024 sc1\n\t" \
  "s_cmp_eq_u32 s49, 0\n\t" \
  "s_cbranch_scc1 La3114_%=\n\t" \
  "s_mov_b32 m0, s50\n\t" \
  "s_sub_u32 s49, s49, 1\n\t" \
  "global_load_lds_dwordx4 v210, s[44:45] sc1\n\t" \
  "global_load_lds_dwordx4 v210, s[44:45] offset:1024 sc1\n\t" \
  "s_add_u32 s44, s44, 2048\n\t" \
  "s_addc_u32 s45, s45, 0\n\t" \
  "s_add_u32 s50, s50, 2048\n\t" \
  "La3114_%=:\n\t" \
  "s_setprio 0\n\t" \
  "v_exp_f32 v32, v32\n\t" \
  "v_exp_f32 v33, v33\n\t" \
  "v_exp_f32 v34, v34\n\t" \
  "v_exp_f32 v35, v35\n\t" \
  "v_exp_f32 v36, v36\n\t" \
  "v_exp_f32 v37, v37\n\t" \
  "v_exp_f32 v38, v38\n\t" \
  "v_exp_f32 v39, v39\n\t" \
  "v_exp_f32 v48, v48\n\t" \
  "v_exp_f32 v49, v49\n\t" \
  "v_exp_f32 v50, v50\n\t" \
  "v_exp_f32 v51, v51\n\t" \
  "v_exp_f32 v52, v52\n\t" \
  "v_exp_f32 v53, v53\n\t" \
  "v_exp_f32 v54, v54\n\t" \
  "v_exp_f32 v55, v55\n\t" \
  "v_exp_f32 v40, v40\n\t" \
  "v_exp_f32 v41, v41\n\t" \
  "v_exp_f32 v42, v42\n\t" \
  "v_exp_f32 v43, v43\n\t" \
  "v_exp_f32 v44, v44\n\t" \
  "v_exp_f32 v45, v45\n\t" \
  "v_exp_f32 v46, v46\n\t" \
  "v_exp_f32 v47, v47\n\t" \
  "s_setprio 1\n\t" \
  "v_exp_f32 v56, v56\n\t" \
  "v_exp_f32 v57, v57\n\t" \
  "v_exp_f32 v58, v58\n\t" \
  "v_exp_f32 v59, v59\n\t" \
  "v_exp_f32 v60, v60\n\t" \
  "v_exp_f32 v61, v61\n\t" \
  "v_exp_f32 v62, v62\n\t" \
  "v_exp_f32 v63, v63\n\t" \
  "s_setprio 3\n\t" \
  "s_waitcnt lgkmcnt(0)\n\t" \
  "v_mfma_f32_32x32x16_bf16 v[0:15], v[104:107], v[110:113], 0\n\t" \
  "v_mfma_f32_32x32x16_bf16 v[16:31], v[104:107], v[114:117], 0\n\t" \
  "v_add_u32 v121, s47, v121\n\t" \
  "ds_read_b128 v[104:107], v121\n\t" \
  "v_smfmac_f32_32x32x32_bf16 v[64:79], v[96:99], v[32:39], v120\n\t" \
  "v_smfmac_f32_32x32x32_bf16 v[80:95], v[96:99], v[48:55], v120\n\t" \
  "ds_read_b128 v[96:99], v123 offset:16128\n\t" \
  "v_mfma_f32_32x32x4_2b_bf16 v[0:31], v[108:109], v[118:119], v[0:31]\n\t" \
  "v_add_u32 v122, s47, v122\n\t" \
  "ds_read_b64 v[108:109], v122\n\t" \
  "v_smfmac_f32_32x32x32_bf16 v[64:79], v[100:103], v[40:47], v120\n\t" \
  "v_smfmac_f32_32x32x32_bf16 v[80:95], v[100:103], v[56:63], v120\n\t" \
  "ds_read_b128 v[100:103], v123 offset:16832\n\t" \
  "s_waitcnt vmcnt(3)\n\t" \
  "v_mfma_f32_32x32x16_bf16 v[128:143], v[180:183], v[176:179], v[128:143]\n\t" \
  "v_mfma_f32_32x32x16_bf16 v[144:159], v[184:187], v[176:179], v[144:159]\n\t" \
  "global_load_dwordx4 v[176:179], v208, s[40:41] offset:2048 sc1\n\t" \
  "global_load_dwordx4 v[180:183], v209, s[40:41] offset:2048 sc1\n\t" \
  "global_load_dwordx4 v[184:187], v210, s[40:41] offset:2048 sc1\n\t" \
  "s_cmp_eq_u32 s49, 0\n\t" \
  "s_cbranch_scc1 La3115_%=\n\t" \
  "s_mov_b32 m0, s50\n\t" \
  "s_sub_u32 s49, s49, 1\n\t" \
  "global_load_lds_dwordx4 v210, s[44:45] sc1\n\t" \
  "global_load_lds_dwordx4 v210, s[44:45] offset:1024 sc1\n\t" \
  "s_add_u32 s44, s44, 2048\n\t" \
  "s_addc_u32 s45, s45, 0\n\t" \
  "s_add_u32 s50, s50, 2048\n\t" \
  "La3115_%=:\n\t" \
  "s_setprio 0\n\t" \
  "v_exp_f32 v0, v0\n\t" \
  "v_exp_f32 v1, v1\n\t" \
  "v_exp_f32 v2, v2\n\t" \
  "v_exp_f32 v3, v3\n\t" \
  "v_exp_f32 v4, v4\n\t" \
  "v_exp_f32 v5, v5\n\t" \
  "v_exp_f32 v6, v6\n\t" \
  "v_exp_f32 v7, v7\n\t" \
  "v_exp_f32 v16, v16\n\t" \
  "v_exp_f32 v17, v17\n\t" \
  "v_exp_f32 v18, v18\n\t" \
  "v_exp_f32 v19, v19\n\t" \
  "v_exp_f32 v20, v20\n\t" \
  "v_exp_f32 v21, v21\n\t" \
  "v_exp_f32 v22, v22\n\t" \
  "v_exp_f32 v23, v23\n\t" \
  "v_exp_f32 v8, v8\n\t" \
  "v_exp_f32 v9, v9\n\t" \
  "v_exp_f32 v10, v10\n\t" \
  "v_exp_f32 v11, v11\n\t" \
  "v_exp_f32 v12, v12\n\t" \
  "v_exp_f32 v13, v13\n\t" \
  "v_exp_f32 v14, v14\n\t" \
  "v_exp_f32 v15, v15\n\t" \
  "s_setprio 1\n\t" \
  "v_exp_f32 v24, v24\n\t" \
  "v_exp_f32 v25, v25\n\t" \
  "v_exp_f32 v26, v26\n\t" \
  "v_exp_f32 v27, v27\n\t" \
  "v_exp_f32 v28, v28\n\t" \
  "v_exp_f32 v29, v29\n\t" \
  "v_exp_f32 v30, v30\n\t" \
  "v_exp_f32 v31, v31\n\t" \
  "s_setprio 3\n\t" \
  "s_waitcnt lgkmcnt(0)\n\t" \
  "v_mfma_f32_32x32x16_bf16 v[32:47], v[104:107], v[110:113], 0\n\t" \
  "v_mfma_f32_32x32x16_bf16 v[48:63], v[104:107], v[114:117], 0\n\t" \
  "ds_read_b128 v[104:107], v121 offset:5376\n\t" \
  "v_smfmac_f32_32x32x32_bf16 v[64:79], v[96:99], v[0:7], v120\n\t" \
  "v_smfmac_f32_32x32x32_bf16 v[80:95], v[96:99], v[16:23], v120\n\t" \
  "v_add_u32 v123, s47, v123\n\t" \
  "ds_read_b128 v[96:99], v123\n\t" \
  "v_mfma_f32_32x32x4_2b_bf16 v[32:63], v[108:109], v[118:119], v[32:63]\n\t" \
  "ds_read_b64 v[108:109], v122 offset:5376\n\t" \
  "v_smfmac_f32_32x32x32_bf16 v[64:79], v[100:103], v[8:15], v120\n\t" \
  "v_smfmac_f32_32x32x32_bf16 v[80:95], v[100:103], v[24:31], v120\n\t" \
  "ds_read_b128 v[100:103], v123 offset:704\n\t" \
  "s_waitcnt vmcnt(3)\n\t" \
  "v_mfma_f32_32x32x16_bf16 v[128:143], v[196:199], v[192:195], v[128:143]\n\t" \
  "v_mfma_f32_32x32x16_bf16 v[144:159], v[200:203], v[192:195], v[144:159]\n\t" \
  "global_load_dwordx4 v[192:195], v208, s[40:41] offset:3072 sc1\n\t" \
  "global_load_dwordx4 v[196:199], v209, s[40:41] offset:3072 sc1\n\t" \
  "global_load_dwordx4 v[200:203], v210, s[40:41] offset:3072 sc1\n\t" \
  "s_add_u32 s40, s40, 4096\n\t" \
  "s_addc_u32 s41, s41, 0\n\t" \
  "s_cmp_eq_u32 s49, 0\n\t" \
  "s_cbranch_scc1 La3116_%=\n\t" \
  "s_mov_b32 m0, s50\n\t" \
  "s_sub_u32 s49, s49, 1\n\t" \
  "global_load_lds_dwordx4 v210, s[44:45] sc1\n\t" \
  "global_load_lds_dwordx4 v210, s[44:45] offset:1024 sc1\n\t" \
  "s_add_u32 s44, s44, 2048\n\t" \
  "s_addc_u32 s45, s45, 0\n\t" \
  "s_add_u32 s50, s50, 2048\n\t" \
  "La3116_%=:\n\t" \
  "s_add_u32 s46, s46, 1\n\t" \
  "s_cmp_lt_u32 s46, 8\n\t" \
  "s_cbranch_scc1 loopa317_%=\n\t" \
  "s_nop 7\n\t" \
  "s_nop 3\n\t" \
  "ds_write_b128 v124, v[64:67]\n\t" \
  "ds_write_b128 v124, v[68:71] offset:1024\n\t" \
  "ds_write_b128 v124, v[72:75] offset:2048\n\t" \
  "s_waitcnt vmcnt(6) lgkmcnt(0)\n\t" \
  "s_barrier\n\t" \
  "ds_read_b128 v[0:3], v125\n\t" \
  "ds_read_b128 v[4:7], v125 offset:1024\n\t" \
  "ds_read_b128 v[8:11], v125 offset:2048\n\t" \
  "s_waitcnt lgkmcnt(2)\n\t" \
  "v_add_f32 v0, v0, v80\n\t" \
  "v_add_f32 v1, v1, v81\n\t" \
  "v_add_f32 v2, v2, v82\n\t" \
  "v_add_f32 v3, v3, v83\n\t" \
  "s_waitcnt lgkmcnt(1)\n\t" \
  "v_add_f32 v4, v4, v84\n\t" \
  "v_add_f32 v5, v5, v85\n\t" \
  "v_add_f32 v6, v6, v86\n\t" \
  "v_add_f32 v7, v7, v87\n\t" \
  "s_waitcnt lgkmcnt(0)\n\t" \
  "v_add_f32 v8, v8, v88\n\t" \
  "v_add_f32 v9, v9, v89\n\t" \
  "v_add_f32 v10, v10, v90\n\t" \
  "v_add_f32 v11, v11, v91\n\t" \
  "s_setprio 0\n\t" \
  "Ea3_%=:\n\t"
#define A4W \
  "s_nop 1\n\t" \
  "ds_read_b128 v[0:3], %[wa]\n\t" \
  "ds_read_b128 v[4:7], %[wa] offset:40960\n\t" \
  "ds_read_b128 v[8:11], %[wa] offset:1024\n\t" \
  "ds_read_b128 v[12:15], %[wa] offset:41984\n\t" \
  "ds_read_b128 v[16:19], %[wa] offset:2048\n\t" \
  "ds_read_b128 v[20:23], %[wa] offset:43008\n\t" \
  "ds_read_b128 v[24:27], %[wa] offset:3072\n\t" \
  "ds_read_b128 v[28:31], %[wa] offset:44032\n\t" \
  "ds_read_b128 v[32:35], %[wa] offset:4096\n\t" \
  "ds_read_b128 v[36:39], %[wa] offset:45056\n\t" \
  "ds_read_b128 v[40:43], %[wa] offset:5120\n\t" \
  "ds_read_b128 v[44:47], %[wa] offset:46080\n\t" \
  "ds_read_b128 v[48:51], %[wa] offset:6144\n\t" \
  "ds_read_b128 v[52:55], %[wa] offset:47104\n\t" \
  "ds_read_b128 v[56:59], %[wa] offset:7168\n\t" \
  "ds_read_b128 v[60:63], %[wa] offset:48128\n\t" \
  "ds_read_b128 v[64:67], %[wa] offset:8192\n\t" \
  "ds_read_b128 v[68:71], %[wa] offset:49152\n\t" \
  "ds_read_b128 v[72:75], %[wa] offset:9216\n\t" \
  "ds_read_b128 v[76:79], %[wa] offset:50176\n\t"
#define A4 \
  "s_nop 4\n\t" \
  "s_bitcmp1_b32 %[fl], 2\n\t" \
  "s_cbranch_scc1 Sa40_%=\n\t" \
  "s_add_u32 s40, s40, 4096\n\t" \
  "s_addc_u32 s41, s41, 0\n\t" \
  "s_add_u32 s52, s40, 4096\n\t" \
  "s_addc_u32 s53, s41, 0\n\t" \
  "s_mov_b32 s48, 0x100000\n\t" \
  "v_sub_f32 v213, 1.0, v213\n\t" \
  "s_waitcnt vmcnt(2)\n\t" \
  "v_mfma_f32_32x32x16_bf16 v[128:143], v[180:183], v[176:179], v[128:143]\n\t" \
  "v_mfma_f32_32x32x16_bf16 v[144:159], v[184:187], v[176:179], v[144:159]\n\t" \
  "v_mfma_f32_32x32x16_bf16 v[160:175], v[188:191], v[176:179], v[160:175]\n\t" \
  "v_mfma_f32_32x32x16_bf16 v[128:143], v[196:199], v[192:195], v[128:143]\n\t" \
  "v_mfma_f32_32x32x16_bf16 v[144:159], v[200:203], v[192:195], v[144:159]\n\t" \
  "v_mfma_f32_32x32x16_bf16 v[160:175], v[204:207], v[192:195], v[160:175]\n\t" \
  "s_nop 7\n\t" \
  "s_nop 3\n\t" \
  "v_mul_f32 v128, v213, v128\n\t" \
  "v_mul_f32 v129, v213, v129\n\t" \
  "v_mul_f32 v130, v213, v130\n\t" \
  "v_mul_f32 v131, v213, v131\n\t" \
  "v_mul_f32 v132, v213, v132\n\t" \
  "v_mul_f32 v133, v213, v133\n\t" \
  "v_mul_f32 v134, v213, v134\n\t" \
  "v_mul_f32 v135, v213, v135\n\t" \
  "v_mul_f32 v136, v213, v136\n\t" \
  "v_mul_f32 v137, v213, v137\n\t" \
  "v_mul_f32 v138, v213, v138\n\t" \
  "v_mul_f32 v139, v213, v139\n\t" \
  "v_mul_f32 v140, v213, v140\n\t" \
  "v_mul_f32 v141, v213, v141\n\t" \
  "v_mul_f32 v142, v213, v142\n\t" \
  "v_mul_f32 v143, v213, v143\n\t" \
  "v_mul_f32 v144, v213, v144\n\t" \
  "v_mul_f32 v145, v213, v145\n\t" \
  "v_mul_f32 v146, v213, v146\n\t" \
  "v_mul_f32 v147, v213, v147\n\t" \
  "v_mul_f32 v148, v213, v148\n\t" \
  "v_mul_f32 v149, v213, v149\n\t" \
  "v_mul_f32 v150, v213, v150\n\t" \
  "v_mul_f32 v151, v213, v151\n\t" \
  "v_mul_f32 v152, v213, v152\n\t" \
  "v_mul_f32 v153, v213, v153\n\t" \
  "v_mul_f32 v154, v213, v154\n\t" \
  "v_mul_f32 v155, v213, v155\n\t" \
  "v_mul_f32 v156, v213, v156\n\t" \
  "v_mul_f32 v157, v213, v157\n\t" \
  "v_mul_f32 v158, v213, v158\n\t" \
  "v_mul_f32 v159, v213, v159\n\t" \
  "v_mul_f32 v160, v213, v160\n\t" \
  "v_mul_f32 v161, v213, v161\n\t" \
  "v_mul_f32 v162, v213, v162\n\t" \
  "v_mul_f32 v163, v213, v163\n\t" \
  "v_mul_f32 v164, v213, v164\n\t" \
  "v_mul_f32 v165, v213, v165\n\t" \
  "v_mul_f32 v166, v213, v166\n\t" \
  "v_mul_f32 v167, v213, v167\n\t" \
  "v_mul_f32 v168, v213, v168\n\t" \
  "v_mul_f32 v169, v213, v169\n\t" \
  "v_mul_f32 v170, v213, v170\n\t" \
  "v_mul_f32 v171, v213, v171\n\t" \
  "v_mul_f32 v172, v213, v172\n\t" \
  "v_mul_f32 v173, v213, v173\n\t" \
  "v_mul_f32 v174, v213, v174\n\t" \
  "v_mul_f32 v175, v213, v175\n\t" \
  "s_waitcnt lgkmcnt(0)\n\t" \
  "s_barrier\n\t" \
  "global_load_dwordx4 v[80:83], v120, s[40:41] offset:-4096 sc1\n\t" \
  "global_load_dwordx4 v[84:87], v120, s[40:41] offset:-3072 sc1\n\t" \
  "global_load_dwordx4 v[88:91], v120, s[40:41] offset:-2048 sc1\n\t" \
  "global_load_dwordx4 v[92:95], v120, s[40:41] offset:-1024 sc1\n\t" \
  "global_load_dwordx4 v[96:99], v120, s[40:41] sc1\n\t" \
  "global_load_dwordx4 v[100:103], v120, s[40:41] offset:1024 sc1\n\t" \
  "global_load_dwordx4 v[104:107], v120, s[40:41] offset:2048 sc1\n\t" \
  "global_load_dwordx4 v[108:111], v120, s[40:41] offset:3072 sc1\n\t" \
  "global_load_dwordx4 v[112:115], v120, s[52:53] sc1\n\t" \
  "global_load_dwordx4 v[116:119], v120, s[52:53] offset:1024 sc1\n\t" \
  "oca401_%=:\n\t" \
  "s_waitcnt vmcnt(9)\n\t" \
  "v_max3_u32 v214, v80, v81, v82\n\t" \
  "v_max_u32 v214, v214, v83\n\t" \
  "v_cmp_eq_u32 vcc, -1, v214\n\t" \
  "s_cbranch_vccnz ora403_%=\n\t" \
  "oga402_%=:\n\t" \
  "v_mfma_f32_32x32x16_bf16 v[128:143], v[0:3], v[80:83], v[128:143]\n\t" \
  "v_mfma_f32_32x32x16_bf16 v[144:159], v[4:7], v[80:83], v[144:159]\n\t" \
  "v_mfma_f32_32x32x16_bf16 v[160:175], v[216:219], v[80:83], v[160:175]\n\t" \
  "oca404_%=:\n\t" \
  "s_waitcnt vmcnt(8)\n\t" \
  "v_max3_u32 v214, v84, v85, v86\n\t" \
  "v_max_u32 v214, v214, v87\n\t" \
  "v_cmp_eq_u32 vcc, -1, v214\n\t" \
  "s_cbranch_vccnz ora406_%=\n\t" \
  "oga405_%=:\n\t" \
  "v_mfma_f32_32x32x16_bf16 v[128:143], v[8:11], v[84:87], v[128:143]\n\t" \
  "v_mfma_f32_32x32x16_bf16 v[144:159], v[12:15], v[84:87], v[144:159]\n\t" \
  "v_mfma_f32_32x32x16_bf16 v[160:175], v[220:223], v[84:87], v[160:175]\n\t" \
  "oca407_%=:\n\t" \
  "s_waitcnt vmcnt(7)\n\t" \
  "v_max3_u32 v214, v88, v89, v90\n\t" \
  "v_max_u32 v214, v214, v91\n\t" \
  "v_cmp_eq_u32 vcc, -1, v214\n\t" \
  "s_cbranch_vccnz ora409_%=\n\t" \
  "oga408_%=:\n\t" \
  "v_mfma_f32_32x32x16_bf16 v[128:143], v[16:19], v[88:91], v[128:143]\n\t" \
  "v_mfma_f32_32x32x16_bf16 v[144:159], v[20:23], v[88:91], v[144:159]\n\t" \
  "v_mfma_f32_32x32x16_bf16 v[160:175], v[224:227], v[88:91], v[160:175]\n\t" \
  "oca4010_%=:\n\t" \
  "s_waitcnt vmcnt(6)\n\t" \
  "v_max3_u32 v214, v92, v93, v94\n\t" \
  "v_max_u32 v214, v214, v95\n\t" \
  "v_cmp_eq_u32 vcc, -1, v214\n\t" \
  "s_cbranch_vccnz ora4012_%=\n\t" \
  "oga4011_%=:\n\t" \
  "v_mfma_f32_32x32x16_bf16 v[128:143], v[24:27], v[92:95], v[128:143]\n\t" \
  "v_mfma_f32_32x32x16_bf16 v[144:159], v[28:31], v[92:95], v[144:159]\n\t" \
  "v_mfma_f32_32x32x16_bf16 v[160:175], v[228:231], v[92:95], v[160:175]\n\t" \
  "oca4013_%=:\n\t" \
  "s_waitcnt vmcnt(5)\n\t" \
  "v_max3_u32 v214, v96, v97, v98\n\t" \
  "v_max_u32 v214, v214, v99\n\t" \
  "v_cmp_eq_u32 vcc, -1, v214\n\t" \
  "s_cbranch_vccnz ora4015_%=\n\t" \
  "oga4014_%=:\n\t" \
  "v_mfma_f32_32x32x16_bf16 v[128:143], v[32:35], v[96:99], v[128:143]\n\t" \
  "v_mfma_f32_32x32x16_bf16 v[144:159], v[36:39], v[96:99], v[144:159]\n\t" \
  "v_mfma_f32_32x32x16_bf16 v[160:175], v[232:235], v[96:99], v[160:175]\n\t" \
  "oca4016_%=:\n\t" \
  "s_waitcnt vmcnt(4)\n\t" \
  "v_max3_u32 v214, v100, v101, v102\n\t" \
  "v_max_u32 v214, v214, v103\n\t" \
  "v_cmp_eq_u32 vcc, -1, v214\n\t" \
  "s_cbranch_vccnz ora4018_%=\n\t" \
  "oga4017_%=:\n\t" \
  "v_mfma_f32_32x32x16_bf16 v[128:143], v[40:43], v[100:103], v[128:143]\n\t" \
  "v_mfma_f32_32x32x16_bf16 v[144:159], v[44:47], v[100:103], v[144:159]\n\t" \
  "v_mfma_f32_32x32x16_bf16 v[160:175], v[236:239], v[100:103], v[160:175]\n\t" \
  "oca4019_%=:\n\t" \
  "s_waitcnt vmcnt(3)\n\t" \
  "v_max3_u32 v214, v104, v105, v106\n\t" \
  "v_max_u32 v214, v214, v107\n\t" \
  "v_cmp_eq_u32 vcc, -1, v214\n\t" \
  "s_cbranch_vccnz ora4021_%=\n\t" \
  "oga4020_%=:\n\t" \
  "v_mfma_f32_32x32x16_bf16 v[128:143], v[48:51], v[104:107], v[128:143]\n\t" \
  "v_mfma_f32_32x32x16_bf16 v[144:159], v[52:55], v[104:107], v[144:159]\n\t" \
  "v_mfma_f32_32x32x16_bf16 v[160:175], v[240:243], v[104:107], v[160:175]\n\t" \
  "oca4022_%=:\n\t" \
  "s_waitcnt vmcnt(2)\n\t" \
  "v_max3_u32 v214, v108, v109, v110\n\t" \
  "v_max_u32 v214, v214, v111\n\t" \
  "v_cmp_eq_u32 vcc, -1, v214\n\t" \
  "s_cbranch_vccnz ora4024_%=\n\t" \
  "oga4023_%=:\n\t" \
  "v_mfma_f32_32x32x16_bf16 v[128:143], v[56:59], v[108:111], v[128:143]\n\t" \
  "v_mfma_f32_32x32x16_bf16 v[144:159], v[60:63], v[108:111], v[144:159]\n\t" \
  "v_mfma_f32_32x32x16_bf16 v[160:175], v[244:247], v[108:111], v[160:175]\n\t" \
  "oca4025_%=:\n\t" \
  "s_waitcnt vmcnt(1)\n\t" \
  "v_max3_u32 v214, v112, v113, v114\n\t" \
  "v_max_u32 v214, v214, v115\n\t" \
  "v_cmp_eq_u32 vcc, -1, v214\n\t" \
  "s_cbranch_vccnz ora4027_%=\n\t" \
  "oga4026_%=:\n\t" \
  "v_mfma_f32_32x32x16_bf16 v[128:143], v[64:67], v[112:115], v[128:143]\n\t" \
  "v_mfma_f32_32x32x16_bf16 v[144:159], v[68:71], v[112:115], v[144:159]\n\t" \
  "v_mfma_f32_32x32x16_bf16 v[160:175], v[248:251], v[112:115], v[160:175]\n\t" \
  "oca4028_%=:\n\t" \
  "s_waitcnt vmcnt(0)\n\t" \
  "v_max3_u32 v214, v116, v117, v118\n\t" \
  "v_max_u32 v214, v214, v119\n\t" \
  "v_cmp_eq_u32 vcc, -1, v214\n\t" \
  "s_cbranch_vccnz ora4030_%=\n\t" \
  "oga4029_%=:\n\t" \
  "v_mfma_f32_32x32x16_bf16 v[128:143], v[72:75], v[116:119], v[128:143]\n\t" \
  "v_mfma_f32_32x32x16_bf16 v[144:159], v[76:79], v[116:119], v[144:159]\n\t" \
  "v_mfma_f32_32x32x16_bf16 v[160:175], v[252:255], v[116:119], v[160:175]\n\t" \
  "s_nop 3\n\t" \
  "ds_write_b128 v123, v[128:131]\n\t" \
  "ds_write_b128 v123, v[132:135] offset:1024\n\t" \
  "ds_write_b128 v123, v[136:139] offset:8192\n\t" \
  "ds_write_b128 v123, v[140:143] offset:9216\n\t" \
  "ds_write_b128 v123, v[144:147] offset:16384\n\t" \
  "ds_write_b128 v123, v[148:151] offset:17408\n\t" \
  "ds_write_b128 v123, v[152:155] offset:24576\n\t" \
  "ds_write_b128 v123, v[156:159] offset:25600\n\t" \
  "s_nop 7\n\t" \
  "s_nop 3\n\t" \
  "ds_write_b128 v123, v[160:163] offset:32768\n\t" \
  "ds_write_b128 v123, v[164:167] offset:33792\n\t" \
  "ds_write_b128 v123, v[168:171] offset:40960\n\t" \
  "ds_write_b128 v123, v[172:175] offset:41984\n\t" \
  "s_waitcnt lgkmcnt(0)\n\t" \
  "s_barrier\n\t" \
  "ds_read_b128 v[0:3], v124\n\t" \
  "ds_read_b128 v[4:7], v124 offset:1024\n\t" \
  "ds_read_b128 v[8:11], v124 offset:2048\n\t" \
  "ds_read_b128 v[12:15], v124 offset:3072\n\t" \
  "ds_read_b128 v[16:19], v124 offset:4096\n\t" \
  "ds_read_b128 v[20:23], v124 offset:5120\n\t" \
  "ds_read_b128 v[24:27], v124 offset:6144\n\t" \
  "ds_read_b128 v[28:31], v124 offset:7168\n\t" \
  "s_waitcnt lgkmcnt(4)\n\t" \
  "v_add_f32 v0, v0, v8\n\t" \
  "v_add_f32 v1, v1, v9\n\t" \
  "v_add_f32 v2, v2, v10\n\t" \
  "v_add_f32 v3, v3, v11\n\t" \
  "v_add_f32 v4, v4, v12\n\t" \
  "v_add_f32 v5, v5, v13\n\t" \
  "v_add_f32 v6, v6, v14\n\t" \
  "v_add_f32 v7, v7, v15\n\t" \
  "s_waitcnt lgkmcnt(0)\n\t" \
  "v_add_f32 v16, v16, v24\n\t" \
  "v_add_f32 v17, v17, v25\n\t" \
  "v_add_f32 v18, v18, v26\n\t" \
  "v_add_f32 v19, v19, v27\n\t" \
  "v_add_f32 v20, v20, v28\n\t" \
  "v_add_f32 v21, v21, v29\n\t" \
  "v_add_f32 v22, v22, v30\n\t" \
  "v_add_f32 v23, v23, v31\n\t" \
  "v_add_f32 v0, v0, v16\n\t" \
  "v_add_f32 v1, v1, v17\n\t" \
  "v_add_f32 v2, v2, v18\n\t" \
  "v_add_f32 v3, v3, v19\n\t" \
  "v_add_f32 v4, v4, v20\n\t" \
  "v_add_f32 v5, v5, v21\n\t" \
  "v_add_f32 v6, v6, v22\n\t" \
  "v_add_f32 v7, v7, v23\n\t" \
  "v_cvt_pk_bf16_f32 v32, v0, v1\n\t" \
  "v_cvt_pk_bf16_f32 v33, v2, v3\n\t" \
  "v_cvt_pk_bf16_f32 v34, v4, v5\n\t" \
  "v_cvt_pk_bf16_f32 v35, v6, v7\n\t" \
  "global_store_dwordx4 v125, v[32:35], %[out]\n\t" \
  "s_branch enda4031_%=\n\t" \
  "ora403_%=:\n\t" \
  "s_cmp_eq_u32 s48, 0\n\t" \
  "s_cbranch_scc1 oga402_%=\n\t" \
  "s_sub_u32 s48, s48, 1\n\t" \
  "global_load_dwordx4 v[80:83], v120, s[40:41] offset:-4096 sc1\n\t" \
  "global_load_dwordx4 v[84:87], v120, s[40:41] offset:-3072 sc1\n\t" \
  "global_load_dwordx4 v[88:91], v120, s[40:41] offset:-2048 sc1\n\t" \
  "global_load_dwordx4 v[92:95], v120, s[40:41] offset:-1024 sc1\n\t" \
  "global_load_dwordx4 v[96:99], v120, s[40:41] sc1\n\t" \
  "global_load_dwordx4 v[100:103], v120, s[40:41] offset:1024 sc1\n\t" \
  "global_load_dwordx4 v[104:107], v120, s[40:41] offset:2048 sc1\n\t" \
  "global_load_dwordx4 v[108:111], v120, s[40:41] offset:3072 sc1\n\t" \
  "global_load_dwordx4 v[112:115], v120, s[52:53] sc1\n\t" \
  "global_load_dwordx4 v[116:119], v120, s[52:53] offset:1024 sc1\n\t" \
  "s_branch oca401_%=\n\t" \
  "ora406_%=:\n\t" \
  "s_cmp_eq_u32 s48, 0\n\t" \
  "s_cbranch_scc1 oga405_%=\n\t" \
  "s_sub_u32 s48, s48, 1\n\t" \
  "global_load_dwordx4 v[84:87], v120, s[40:41] offset:-3072 sc1\n\t" \
  "global_load_dwordx4 v[88:91], v120, s[40:41] offset:-2048 sc1\n\t" \
  "global_load_dwordx4 v[92:95], v120, s[40:41] offset:-1024 sc1\n\t" \
  "global_load_dwordx4 v[96:99], v120, s[40:41] sc1\n\t" \
  "global_load_dwordx4 v[100:103], v120, s[40:41] offset:1024 sc1\n\t" \
  "global_load_dwordx4 v[104:107], v120, s[40:41] offset:2048 sc1\n\t" \
  "global_load_dwordx4 v[108:111], v120, s[40:41] offset:3072 sc1\n\t" \
  "global_load_dwordx4 v[112:115], v120, s[52:53] sc1\n\t" \
  "global_load_dwordx4 v[116:119], v120, s[52:53] offset:1024 sc1\n\t" \
  "s_branch oca404_%=\n\t" \
  "ora409_%=:\n\t" \
  "s_cmp_eq_u32 s48, 0\n\t" \
  "s_cbranch_scc1 oga408_%=\n\t" \
  "s_sub_u32 s48, s48, 1\n\t" \
  "global_load_dwordx4 v[88:91], v120, s[40:41] offset:-2048 sc1\n\t" \
  "global_load_dwordx4 v[92:95], v120, s[40:41] offset:-1024 sc1\n\t" \
  "global_load_dwordx4 v[96:99], v120, s[40:41] sc1\n\t" \
  "global_load_dwordx4 v[100:103], v120, s[40:41] offset:1024 sc1\n\t" \
  "global_load_dwordx4 v[104:107], v120, s[40:41] offset:2048 sc1\n\t" \
  "global_load_dwordx4 v[108:111], v120, s[40:41] offset:3072 sc1\n\t" \
  "global_load_dwordx4 v[112:115], v120, s[52:53] sc1\n\t" \
  "global_load_dwordx4 v[116:119], v120, s[52:53] offset:1024 sc1\n\t" \
  "s_branch oca407_%=\n\t" \
  "ora4012_%=:\n\t" \
  "s_cmp_eq_u32 s48, 0\n\t" \
  "s_cbranch_scc1 oga4011_%=\n\t" \
  "s_sub_u32 s48, s48, 1\n\t" \
  "global_load_dwordx4 v[92:95], v120, s[40:41] offset:-1024 sc1\n\t" \
  "global_load_dwordx4 v[96:99], v120, s[40:41] sc1\n\t" \
  "global_load_dwordx4 v[100:103], v120, s[40:41] offset:1024 sc1\n\t" \
  "global_load_dwordx4 v[104:107], v120, s[40:41] offset:2048 sc1\n\t" \
  "global_load_dwordx4 v[108:111], v120, s[40:41] offset:3072 sc1\n\t" \
  "global_load_dwordx4 v[112:115], v120, s[52:53] sc1\n\t" \
  "global_load_dwordx4 v[116:119], v120, s[52:53] offset:1024 sc1\n\t" \
  "s_branch oca4010_%=\n\t" \
  "ora4015_%=:\n\t" \
  "s_cmp_eq_u32 s48, 0\n\t" \
  "s_cbranch_scc1 oga4014_%=\n\t" \
  "s_sub_u32 s48, s48, 1\n\t" \
  "global_load_dwordx4 v[96:99], v120, s[40:41] sc1\n\t" \
  "global_load_dwordx4 v[100:103], v120, s[40:41] offset:1024 sc1\n\t" \
  "global_load_dwordx4 v[104:107], v120, s[40:41] offset:2048 sc1\n\t" \
  "global_load_dwordx4 v[108:111], v120, s[40:41] offset:3072 sc1\n\t" \
  "global_load_dwordx4 v[112:115], v120, s[52:53] sc1\n\t" \
  "global_load_dwordx4 v[116:119], v120, s[52:53] offset:1024 sc1\n\t" \
  "s_branch oca4013_%=\n\t" \
  "ora4018_%=:\n\t" \
  "s_cmp_eq_u32 s48, 0\n\t" \
  "s_cbranch_scc1 oga4017_%=\n\t" \
  "s_sub_u32 s48, s48, 1\n\t" \
  "global_load_dwordx4 v[100:103], v120, s[40:41] offset:1024 sc1\n\t" \
  "global_load_dwordx4 v[104:107], v120, s[40:41] offset:2048 sc1\n\t" \
  "global_load_dwordx4 v[108:111], v120, s[40:41] offset:3072 sc1\n\t" \
  "global_load_dwordx4 v[112:115], v120, s[52:53] sc1\n\t" \
  "global_load_dwordx4 v[116:119], v120, s[52:53] offset:1024 sc1\n\t" \
  "s_branch oca4016_%=\n\t" \
  "ora4021_%=:\n\t" \
  "s_cmp_eq_u32 s48, 0\n\t" \
  "s_cbranch_scc1 oga4020_%=\n\t" \
  "s_sub_u32 s48, s48, 1\n\t" \
  "global_load_dwordx4 v[104:107], v120, s[40:41] offset:2048 sc1\n\t" \
  "global_load_dwordx4 v[108:111], v120, s[40:41] offset:3072 sc1\n\t" \
  "global_load_dwordx4 v[112:115], v120, s[52:53] sc1\n\t" \
  "global_load_dwordx4 v[116:119], v120, s[52:53] offset:1024 sc1\n\t" \
  "s_branch oca4019_%=\n\t" \
  "ora4024_%=:\n\t" \
  "s_cmp_eq_u32 s48, 0\n\t" \
  "s_cbranch_scc1 oga4023_%=\n\t" \
  "s_sub_u32 s48, s48, 1\n\t" \
  "global_load_dwordx4 v[108:111], v120, s[40:41] offset:3072 sc1\n\t" \
  "global_load_dwordx4 v[112:115], v120, s[52:53] sc1\n\t" \
  "global_load_dwordx4 v[116:119], v120, s[52:53] offset:1024 sc1\n\t" \
  "s_branch oca4022_%=\n\t" \
  "ora4027_%=:\n\t" \
  "s_cmp_eq_u32 s48, 0\n\t" \
  "s_cbranch_scc1 oga4026_%=\n\t" \
  "s_sub_u32 s48, s48, 1\n\t" \
  "global_load_dwordx4 v[112:115], v120, s[52:53] sc1\n\t" \
  "global_load_dwordx4 v[116:119], v120, s[52:53] offset:1024 sc1\n\t" \
  "s_branch oca4025_%=\n\t" \
  "ora4030_%=:\n\t" \
  "s_cmp_eq_u32 s48, 0\n\t" \
  "s_cbranch_scc1 oga4029_%=\n\t" \
  "s_sub_u32 s48, s48, 1\n\t" \
  "global_load_dwordx4 v[116:119], v120, s[52:53] offset:1024 sc1\n\t" \
  "s_branch oca4028_%=\n\t" \
  "enda4031_%=:\n\t" \
  "s_branch Ea4_%=\n\t" \
  "Sa40_%=:\n\t" \
  "s_add_u32 s40, s40, 4096\n\t" \
  "s_addc_u32 s41, s41, 0\n\t" \
  "s_add_u32 s52, s40, 4096\n\t" \
  "s_addc_u32 s53, s41, 0\n\t" \
  "s_mov_b32 s48, 0x100000\n\t" \
  "v_sub_f32 v213, 1.0, v213\n\t" \
  "s_waitcnt vmcnt(2)\n\t" \
  "v_mfma_f32_32x32x16_bf16 v[128:143], v[180:183], v[176:179], v[128:143]\n\t" \
  "v_mfma_f32_32x32x16_bf16 v[144:159], v[184:187], v[176:179], v[144:159]\n\t" \
  "v_mfma_f32_32x32x16_bf16 v[128:143], v[196:199], v[192:195], v[128:143]\n\t" \
  "v_mfma_f32_32x32x16_bf16 v[144:159], v[200:203], v[192:195], v[144:159]\n\t" \
  "s_nop 7\n\t" \
  "s_nop 3\n\t" \
  "v_mul_f32 v128, v213, v128\n\t" \
  "v_mul_f32 v129, v213, v129\n\t" \
  "v_mul_f32 v130, v213, v130\n\t" \
  "v_mul_f32 v131, v213, v131\n\t" \
  "v_mul_f32 v132, v213, v132\n\t" \
  "v_mul_f32 v133, v213, v133\n\t" \
  "v_mul_f32 v134, v213, v134\n\t" \
  "v_mul_f32 v135, v213, v135\n\t" \
  "v_mul_f32 v136, v213, v136\n\t" \
  "v_mul_f32 v137, v213, v137\n\t" \
  "v_mul_f32 v138, v213, v138\n\t" \
  "v_mul_f32 v139, v213, v139\n\t" \
  "v_mul_f32 v140, v213, v140\n\t" \
  "v_mul_f32 v141, v213, v141\n\t" \
  "v_mul_f32 v142, v213, v142\n\t" \
  "v_mul_f32 v143, v213, v143\n\t" \
  "v_mul_f32 v144, v213, v144\n\t" \
  "v_mul_f32 v145, v213, v145\n\t" \
  "v_mul_f32 v146, v213, v146\n\t" \
  "v_mul_f32 v147, v213, v147\n\t" \
  "v_mul_f32 v148, v213, v148\n\t" \
  "v_mul_f32 v149, v213, v149\n\t" \
  "v_mul_f32 v150, v213, v150\n\t" \
  "v_mul_f32 v151, v213, v151\n\t" \
  "v_mul_f32 v152, v213, v152\n\t" \
  "v_mul_f32 v153, v213, v153\n\t" \
  "v_mul_f32 v154, v213, v154\n\t" \
  "v_mul_f32 v155, v213, v155\n\t" \
  "v_mul_f32 v156, v213, v156\n\t" \
  "v_mul_f32 v157, v213, v157\n\t" \
  "v_mul_f32 v158, v213, v158\n\t" \
  "v_mul_f32 v159, v213, v159\n\t" \
  "s_waitcnt lgkmcnt(0)\n\t" \
  "s_barrier\n\t" \
  "global_load_dwordx4 v[80:83], v120, s[40:41] offset:-4096 sc1\n\t" \
  "global_load_dwordx4 v[84:87], v120, s[40:41] offset:-3072 sc1\n\t" \
  "global_load_dwordx4 v[88:91], v120, s[40:41] offset:-2048 sc1\n\t" \
  "global_load_dwordx4 v[92:95], v120, s[40:41] offset:-1024 sc1\n\t" \
  "global_load_dwordx4 v[96:99], v120, s[40:41] sc1\n\t" \
  "global_load_dwordx4 v[100:103], v120, s[40:41] offset:1024 sc1\n\t" \
  "global_load_dwordx4 v[104:107], v120, s[40:41] offset:2048 sc1\n\t" \
  "global_load_dwordx4 v[108:111], v120, s[40:41] offset:3072 sc1\n\t" \
  "global_load_dwordx4 v[112:115], v120, s[52:53] sc1\n\t" \
  "global_load_dwordx4 v[116:119], v120, s[52:53] offset:1024 sc1\n\t" \
  "oca411_%=:\n\t" \
  "s_waitcnt vmcnt(9)\n\t" \
  "v_max3_u32 v214, v80, v81, v82\n\t" \
  "v_max_u32 v214, v214, v83\n\t" \
  "v_cmp_eq_u32 vcc, -1, v214\n\t" \
  "s_cbranch_vccnz ora413_%=\n\t" \
  "oga412_%=:\n\t" \
  "v_mfma_f32_32x32x16_bf16 v[128:143], v[0:3], v[80:83], v[128:143]\n\t" \
  "v_mfma_f32_32x32x16_bf16 v[144:159], v[4:7], v[80:83], v[144:159]\n\t" \
  "oca414_%=:\n\t" \
  "s_waitcnt vmcnt(8)\n\t" \
  "v_max3_u32 v214, v84, v85, v86\n\t" \
  "v_max_u32 v214, v214, v87\n\t" \
  "v_cmp_eq_u32 vcc, -1, v214\n\t" \
  "s_cbranch_vccnz ora416_%=\n\t" \
  "oga415_%=:\n\t" \
  "v_mfma_f32_32x32x16_bf16 v[128:143], v[8:11], v[84:87], v[128:143]\n\t" \
  "v_mfma_f32_32x32x16_bf16 v[144:159], v[12:15], v[84:87], v[144:159]\n\t" \
  "oca417_%=:\n\t" \
  "s_waitcnt vmcnt(7)\n\t" \
  "v_max3_u32 v214, v88, v89, v90\n\t" \
  "v_max_u32 v214, v214, v91\n\t" \
  "v_cmp_eq_u32 vcc, -1, v214\n\t" \
  "s_cbranch_vccnz ora419_%=\n\t" \
  "oga418_%=:\n\t" \
  "v_mfma_f32_32x32x16_bf16 v[128:143], v[16:19], v[88:91], v[128:143]\n\t" \
  "v_mfma_f32_32x32x16_bf16 v[144:159], v[20:23], v[88:91], v[144:159]\n\t" \
  "oca4110_%=:\n\t" \
  "s_waitcnt vmcnt(6)\n\t" \
  "v_max3_u32 v214, v92, v93, v94\n\t" \
  "v_max_u32 v214, v214, v95\n\t" \
  "v_cmp_eq_u32 vcc, -1, v214\n\t" \
  "s_cbranch_vccnz ora4112_%=\n\t" \
  "oga4111_%=:\n\t" \
  "v_mfma_f32_32x32x16_bf16 v[128:143], v[24:27], v[92:95], v[128:143]\n\t" \
  "v_mfma_f32_32x32x16_bf16 v[144:159], v[28:31], v[92:95], v[144:159]\n\t" \
  "oca4113_%=:\n\t" \
  "s_waitcnt vmcnt(5)\n\t" \
  "v_max3_u32 v214, v96, v97, v98\n\t" \
  "v_max_u32 v214, v214, v99\n\t" \
  "v_cmp_eq_u32 vcc, -1, v214\n\t" \
  "s_cbranch_vccnz ora4115_%=\n\t" \
  "oga4114_%=:\n\t" \
  "v_mfma_f32_32x32x16_bf16 v[128:143], v[32:35], v[96:99], v[128:143]\n\t" \
  "v_mfma_f32_32x32x16_bf16 v[144:159], v[36:39], v[96:99], v[144:159]\n\t" \
  "oca4116_%=:\n\t" \
  "s_waitcnt vmcnt(4)\n\t" \
  "v_max3_u32 v214, v100, v101, v102\n\t" \
  "v_max_u32 v214, v214, v103\n\t" \
  "v_cmp_eq_u32 vcc, -1, v214\n\t" \
  "s_cbranch_vccnz ora4118_%=\n\t" \
  "oga4117_%=:\n\t" \
  "v_mfma_f32_32x32x16_bf16 v[128:143], v[40:43], v[100:103], v[128:143]\n\t" \
  "v_mfma_f32_32x32x16_bf16 v[144:159], v[44:47], v[100:103], v[144:159]\n\t" \
  "oca4119_%=:\n\t" \
  "s_waitcnt vmcnt(3)\n\t" \
  "v_max3_u32 v214, v104, v105, v106\n\t" \
  "v_max_u32 v214, v214, v107\n\t" \
  "v_cmp_eq_u32 vcc, -1, v214\n\t" \
  "s_cbranch_vccnz ora4121_%=\n\t" \
  "oga4120_%=:\n\t" \
  "v_mfma_f32_32x32x16_bf16 v[128:143], v[48:51], v[104:107], v[128:143]\n\t" \
  "v_mfma_f32_32x32x16_bf16 v[144:159], v[52:55], v[104:107], v[144:159]\n\t" \
  "oca4122_%=:\n\t" \
  "s_waitcnt vmcnt(2)\n\t" \
  "v_max3_u32 v214, v108, v109, v110\n\t" \
  "v_max_u32 v214, v214, v111\n\t" \
  "v_cmp_eq_u32 vcc, -1, v214\n\t" \
  "s_cbranch_vccnz ora4124_%=\n\t" \
  "oga4123_%=:\n\t" \
  "v_mfma_f32_32x32x16_bf16 v[128:143], v[56:59], v[108:111], v[128:143]\n\t" \
  "v_mfma_f32_32x32x16_bf16 v[144:159], v[60:63], v[108:111], v[144:159]\n\t" \
  "oca4125_%=:\n\t" \
  "s_waitcnt vmcnt(1)\n\t" \
  "v_max3_u32 v214, v112, v113, v114\n\t" \
  "v_max_u32 v214, v214, v115\n\t" \
  "v_cmp_eq_u32 vcc, -1, v214\n\t" \
  "s_cbranch_vccnz ora4127_%=\n\t" \
  "oga4126_%=:\n\t" \
  "v_mfma_f32_32x32x16_bf16 v[128:143], v[64:67], v[112:115], v[128:143]\n\t" \
  "v_mfma_f32_32x32x16_bf16 v[144:159], v[68:71], v[112:115], v[144:159]\n\t" \
  "oca4128_%=:\n\t" \
  "s_waitcnt vmcnt(0)\n\t" \
  "v_max3_u32 v214, v116, v117, v118\n\t" \
  "v_max_u32 v214, v214, v119\n\t" \
  "v_cmp_eq_u32 vcc, -1, v214\n\t" \
  "s_cbranch_vccnz ora4130_%=\n\t" \
  "oga4129_%=:\n\t" \
  "v_mfma_f32_32x32x16_bf16 v[128:143], v[72:75], v[116:119], v[128:143]\n\t" \
  "v_mfma_f32_32x32x16_bf16 v[144:159], v[76:79], v[116:119], v[144:159]\n\t" \
  "s_nop 7\n\t" \
  "ds_write_b128 v123, v[128:131]\n\t" \
  "ds_write_b128 v123, v[132:135] offset:1024\n\t" \
  "ds_write_b128 v123, v[136:139] offset:8192\n\t" \
  "ds_write_b128 v123, v[140:143] offset:9216\n\t" \
  "s_nop 7\n\t" \
  "s_nop 3\n\t" \
  "ds_write_b128 v123, v[144:147] offset:16384\n\t" \
  "ds_write_b128 v123, v[148:151] offset:17408\n\t" \
  "ds_write_b128 v123, v[152:155] offset:24576\n\t" \
  "ds_write_b128 v123, v[156:159] offset:25600\n\t" \
  "s_waitcnt lgkmcnt(0)\n\t" \
  "s_barrier\n\t" \
  "ds_read_b128 v[0:3], v124\n\t" \
  "ds_read_b128 v[4:7], v124 offset:1024\n\t" \
  "ds_read_b128 v[8:11], v124 offset:2048\n\t" \
  "ds_read_b128 v[12:15], v124 offset:3072\n\t" \
  "ds_read_b128 v[16:19], v124 offset:4096\n\t" \
  "ds_read_b128 v[20:23], v124 offset:5120\n\t" \
  "ds_read_b128 v[24:27], v124 offset:6144\n\t" \
  "ds_read_b128 v[28:31], v124 offset:7168\n\t" \
  "s_waitcnt lgkmcnt(4)\n\t" \
  "v_add_f32 v0, v0, v8\n\t" \
  "v_add_f32 v1, v1, v9\n\t" \
  "v_add_f32 v2, v2, v10\n\t" \
  "v_add_f32 v3, v3, v11\n\t" \
  "v_add_f32 v4, v4, v12\n\t" \
  "v_add_f32 v5, v5, v13\n\t" \
  "v_add_f32 v6, v6, v14\n\t" \
  "v_add_f32 v7, v7, v15\n\t" \
  "s_waitcnt lgkmcnt(0)\n\t" \
  "v_add_f32 v16, v16, v24\n\t" \
  "v_add_f32 v17, v17, v25\n\t" \
  "v_add_f32 v18, v18, v26\n\t" \
  "v_add_f32 v19, v19, v27\n\t" \
  "v_add_f32 v20, v20, v28\n\t" \
  "v_add_f32 v21, v21, v29\n\t" \
  "v_add_f32 v22, v22, v30\n\t" \
  "v_add_f32 v23, v23, v31\n\t" \
  "v_add_f32 v0, v0, v16\n\t" \
  "v_add_f32 v1, v1, v17\n\t" \
  "v_add_f32 v2, v2, v18\n\t" \
  "v_add_f32 v3, v3, v19\n\t" \
  "v_add_f32 v4, v4, v20\n\t" \
  "v_add_f32 v5, v5, v21\n\t" \
  "v_add_f32 v6, v6, v22\n\t" \
  "v_add_f32 v7, v7, v23\n\t" \
  "v_cvt_pk_bf16_f32 v32, v0, v1\n\t" \
  "v_cvt_pk_bf16_f32 v33, v2, v3\n\t" \
  "v_cvt_pk_bf16_f32 v34, v4, v5\n\t" \
  "v_cvt_pk_bf16_f32 v35, v6, v7\n\t" \
  "global_store_dwordx4 v125, v[32:35], %[out]\n\t" \
  "s_cmp_gt_u32 %[w3k], 15360\n\t" \
  "s_cbranch_scc1 La4131_%=\n\t" \
  "v_add_u32 v124, -16384, v124\n\t" \
  "ds_read_b128 v[0:3], v124\n\t" \
  "ds_read_b128 v[4:7], v124 offset:1024\n\t" \
  "ds_read_b128 v[8:11], v124 offset:2048\n\t" \
  "ds_read_b128 v[12:15], v124 offset:3072\n\t" \
  "ds_read_b128 v[16:19], v124 offset:4096\n\t" \
  "ds_read_b128 v[20:23], v124 offset:5120\n\t" \
  "ds_read_b128 v[24:27], v124 offset:6144\n\t" \
  "ds_read_b128 v[28:31], v124 offset:7168\n\t" \
  "s_waitcnt lgkmcnt(4)\n\t" \
  "v_add_f32 v0, v0, v8\n\t" \
  "v_add_f32 v1, v1, v9\n\t" \
  "v_add_f32 v2, v2, v10\n\t" \
  "v_add_f32 v3, v3, v11\n\t" \
  "v_add_f32 v4, v4, v12\n\t" \
  "v_add_f32 v5, v5, v13\n\t" \
  "v_add_f32 v6, v6, v14\n\t" \
  "v_add_f32 v7, v7, v15\n\t" \
  "s_waitcnt lgkmcnt(0)\n\t" \
  "v_add_f32 v16, v16, v24\n\t" \
  "v_add_f32 v17, v17, v25\n\t" \
  "v_add_f32 v18, v18, v26\n\t" \
  "v_add_f32 v19, v19, v27\n\t" \
  "v_add_f32 v20, v20, v28\n\t" \
  "v_add_f32 v21, v21, v29\n\t" \
  "v_add_f32 v22, v22, v30\n\t" \
  "v_add_f32 v23, v23, v31\n\t" \
  "v_add_f32 v0, v0, v16\n\t" \
  "v_add_f32 v1, v1, v17\n\t" \
  "v_add_f32 v2, v2, v18\n\t" \
  "v_add_f32 v3, v3, v19\n\t" \
  "v_add_f32 v4, v4, v20\n\t" \
  "v_add_f32 v5, v5, v21\n\t" \
  "v_add_f32 v6, v6, v22\n\t" \
  "v_add_f32 v7, v7, v23\n\t" \
  "v_cvt_pk_bf16_f32 v32, v0, v1\n\t" \
  "v_cvt_pk_bf16_f32 v33, v2, v3\n\t" \
  "v_cvt_pk_bf16_f32 v34, v4, v5\n\t" \
  "v_cvt_pk_bf16_f32 v35, v6, v7\n\t" \
  "global_store_dwordx4 v127, v[32:35], %[out]\n\t" \
  "La4131_%=:\n\t" \
  "s_branch enda4132_%=\n\t" \
  "ora413_%=:\n\t" \
  "s_cmp_eq_u32 s48, 0\n\t" \
  "s_cbranch_scc1 oga412_%=\n\t" \
  "s_sub_u32 s48, s48, 1\n\t" \
  "global_load_dwordx4 v[80:83], v120, s[40:41] offset:-4096 sc1\n\t" \
  "global_load_dwordx4 v[84:87], v120, s[40:41] offset:-3072 sc1\n\t" \
  "global_load_dwordx4 v[88:91], v120, s[40:41] offset:-2048 sc1\n\t" \
  "global_load_dwordx4 v[92:95], v120, s[40:41] offset:-1024 sc1\n\t" \
  "global_load_dwordx4 v[96:99], v120, s[40:41] sc1\n\t" \
  "global_load_dwordx4 v[100:103], v120, s[40:41] offset:1024 sc1\n\t" \
  "global_load_dwordx4 v[104:107], v120, s[40:41] offset:2048 sc1\n\t" \
  "global_load_dwordx4 v[108:111], v120, s[40:41] offset:3072 sc1\n\t" \
  "global_load_dwordx4 v[112:115], v120, s[52:53] sc1\n\t" \
  "global_load_dwordx4 v[116:119], v120, s[52:53] offset:1024 sc1\n\t" \
  "s_branch oca411_%=\n\t" \
  "ora416_%=:\n\t" \
  "s_cmp_eq_u32 s48, 0\n\t" \
  "s_cbranch_scc1 oga415_%=\n\t" \
  "s_sub_u32 s48, s48, 1\n\t" \
  "global_load_dwordx4 v[84:87], v120, s[40:41] offset:-3072 sc1\n\t" \
  "global_load_dwordx4 v[88:91], v120, s[40:41] offset:-2048 sc1\n\t" \
  "global_load_dwordx4 v[92:95], v120, s[40:41] offset:-1024 sc1\n\t" \
  "global_load_dwordx4 v[96:99], v120, s[40:41] sc1\n\t" \
  "global_load_dwordx4 v[100:103], v120, s[40:41] offset:1024 sc1\n\t" \
  "global_load_dwordx4 v[104:107], v120, s[40:41] offset:2048 sc1\n\t" \
  "global_load_dwordx4 v[108:111], v120, s[40:41] offset:3072 sc1\n\t" \
  "global_load_dwordx4 v[112:115], v120, s[52:53] sc1\n\t" \
  "global_load_dwordx4 v[116:119], v120, s[52:53] offset:1024 sc1\n\t" \
  "s_branch oca414_%=\n\t" \
  "ora419_%=:\n\t" \
  "s_cmp_eq_u32 s48, 0\n\t" \
  "s_cbranch_scc1 oga418_%=\n\t" \
  "s_sub_u32 s48, s48, 1\n\t" \
  "global_load_dwordx4 v[88:91], v120, s[40:41] offset:-2048 sc1\n\t" \
  "global_load_dwordx4 v[92:95], v120, s[40:41] offset:-1024 sc1\n\t" \
  "global_load_dwordx4 v[96:99], v120, s[40:41] sc1\n\t" \
  "global_load_dwordx4 v[100:103], v120, s[40:41] offset:1024 sc1\n\t" \
  "global_load_dwordx4 v[104:107], v120, s[40:41] offset:2048 sc1\n\t" \
  "global_load_dwordx4 v[108:111], v120, s[40:41] offset:3072 sc1\n\t" \
  "global_load_dwordx4 v[112:115], v120, s[52:53] sc1\n\t" \
  "global_load_dwordx4 v[116:119], v120, s[52:53] offset:1024 sc1\n\t" \
  "s_branch oca417_%=\n\t" \
  "ora4112_%=:\n\t" \
  "s_cmp_eq_u32 s48, 0\n\t" \
  "s_cbranch_scc1 oga4111_%=\n\t" \
  "s_sub_u32 s48, s48, 1\n\t" \
  "global_load_dwordx4 v[92:95], v120, s[40:41] offset:-1024 sc1\n\t" \
  "global_load_dwordx4 v[96:99], v120, s[40:41] sc1\n\t" \
  "global_load_dwordx4 v[100:103], v120, s[40:41] offset:1024 sc1\n\t" \
  "global_load_dwordx4 v[104:107], v120, s[40:41] offset:2048 sc1\n\t" \
  "global_load_dwordx4 v[108:111], v120, s[40:41] offset:3072 sc1\n\t" \
  "global_load_dwordx4 v[112:115], v120, s[52:53] sc1\n\t" \
  "global_load_dwordx4 v[116:119], v120, s[52:53] offset:1024 sc1\n\t" \
  "s_branch oca4110_%=\n\t" \
  "ora4115_%=:\n\t" \
  "s_cmp_eq_u32 s48, 0\n\t" \
  "s_cbranch_scc1 oga4114_%=\n\t" \
  "s_sub_u32 s48, s48, 1\n\t" \
  "global_load_dwordx4 v[96:99], v120, s[40:41] sc1\n\t" \
  "global_load_dwordx4 v[100:103], v120, s[40:41] offset:1024 sc1\n\t" \
  "global_load_dwordx4 v[104:107], v120, s[40:41] offset:2048 sc1\n\t" \
  "global_load_dwordx4 v[108:111], v120, s[40:41] offset:3072 sc1\n\t" \
  "global_load_dwordx4 v[112:115], v120, s[52:53] sc1\n\t" \
  "global_load_dwordx4 v[116:119], v120, s[52:53] offset:1024 sc1\n\t" \
  "s_branch oca4113_%=\n\t" \
  "ora4118_%=:\n\t" \
  "s_cmp_eq_u32 s48, 0\n\t" \
  "s_cbranch_scc1 oga4117_%=\n\t" \
  "s_sub_u32 s48, s48, 1\n\t" \
  "global_load_dwordx4 v[100:103], v120, s[40:41] offset:1024 sc1\n\t" \
  "global_load_dwordx4 v[104:107], v120, s[40:41] offset:2048 sc1\n\t" \
  "global_load_dwordx4 v[108:111], v120, s[40:41] offset:3072 sc1\n\t" \
  "global_load_dwordx4 v[112:115], v120, s[52:53] sc1\n\t" \
  "global_load_dwordx4 v[116:119], v120, s[52:53] offset:1024 sc1\n\t" \
  "s_branch oca4116_%=\n\t" \
  "ora4121_%=:\n\t" \
  "s_cmp_eq_u32 s48, 0\n\t" \
  "s_cbranch_scc1 oga4120_%=\n\t" \
  "s_sub_u32 s48, s48, 1\n\t" \
  "global_load_dwordx4 v[104:107], v120, s[40:41] offset:2048 sc1\n\t" \
  "global_load_dwordx4 v[108:111], v120, s[40:41] offset:3072 sc1\n\t" \
  "global_load_dwordx4 v[112:115], v120, s[52:53] sc1\n\t" \
  "global_load_dwordx4 v[116:119], v120, s[52:53] offset:1024 sc1\n\t" \
  "s_branch oca4119_%=\n\t" \
  "ora4124_%=:\n\t" \
  "s_cmp_eq_u32 s48, 0\n\t" \
  "s_cbranch_scc1 oga4123_%=\n\t" \
  "s_sub_u32 s48, s48, 1\n\t" \
  "global_load_dwordx4 v[108:111], v120, s[40:41] offset:3072 sc1\n\t" \
  "global_load_dwordx4 v[112:115], v120, s[52:53] sc1\n\t" \
  "global_load_dwordx4 v[116:119], v120, s[52:53] offset:1024 sc1\n\t" \
  "s_branch oca4122_%=\n\t" \
  "ora4127_%=:\n\t" \
  "s_cmp_eq_u32 s48, 0\n\t" \
  "s_cbranch_scc1 oga4126_%=\n\t" \
  "s_sub_u32 s48, s48, 1\n\t" \
  "global_load_dwordx4 v[112:115], v120, s[52:53] sc1\n\t" \
  "global_load_dwordx4 v[116:119], v120, s[52:53] offset:1024 sc1\n\t" \
  "s_branch oca4125_%=\n\t" \
  "ora4130_%=:\n\t" \
  "s_cmp_eq_u32 s48, 0\n\t" \
  "s_cbranch_scc1 oga4129_%=\n\t" \
  "s_sub_u32 s48, s48, 1\n\t" \
  "global_load_dwordx4 v[116:119], v120, s[52:53] offset:1024 sc1\n\t" \
  "s_branch oca4128_%=\n\t" \
  "enda4132_%=:\n\t" \
  "Ea4_%=:\n\t" \
  ".Lcode_end:\n\t"

#define IDS(b, w)                                                                                                     \
  const unsigned xcd = (b) & 7, idx = (b) >> 3, r = (xcd & 1) * 8 + (idx & 7), g = (xcd >> 1) * 4 + (idx >> 3);       \
  const unsigned rw = (w) & 3, cw = (w) >> 2, T1 = 4 * r + rw, rep = (xcd * 2 + (idx & 1)) * 1024;                    \
  const unsigned head = xcd * 4 + (idx & 3), qk = idx >> 2, odd = head & 1, hp = head >> 1;                           \
  const unsigned rp2 = 4 * xcd + (idx >> 3), cr = (idx >> 1) & 3, half = idx & 1;                                     \
  const unsigned ra = 2 * rp2 + half, c0 = 5 * cr + 3 * half, c2 = 5 * cr + 2, Tq = 8 * qk + 2 * rw, Tf = ra ^ cw

__global__ __launch_bounds__(512, 2) void k_main(const char* x, const char* wqkv, const char* wo, const char* w1, const char* b1,
                                                 const char* w2, const char* b2, const char* gw, char* ws, char* out) {
  const unsigned b = blockIdx.x, lane = threadIdx.x & 63;
  const unsigned w = __builtin_amdgcn_readfirstlane(threadIdx.x >> 6);
  IDS(b, w);
  const unsigned t = lane & 31, hh = lane >> 5, role = (w + 1) >> 3;
  const unsigned fl = __builtin_amdgcn_readfirstlane(role | (b == LEAD ? 32 + 2 * role : 0) | (w & 4) | ((g + 6) >> 4 << 3) | ((g + 1) & 16));
  char* const fb = ws + WS_F;
  STAMP(0);
  CYC0();

  const unsigned ox0 = (16 * w + (lane >> 3)) * 1280 + (((lane & 7) ^ (lane >> 4)) << 4);
  const unsigned ox1 = (16 * w + 8 + (lane >> 3)) * 1280 + (((lane & 7) ^ (4 + (lane >> 4))) << 4) - 1024;
  const unsigned oq0 = (8 * w + (lane >> 4)) * 3840 + (((lane & 15) ^ ((lane >> 4) << 2)) << 4);
  u32x4 dma = {ox0, ox1, oq0, oq0 + 4 * 3840 - 1024};
  const char* xb = x + r * 128 * 1280;
  const char* qb = wqkv + g * 256;
  const unsigned w2k = w * 2048;
  asm volatile(A0
               : "+{s[40:41]}"(xb), "+{s[42:43]}"(qb)
               : "{v[64:67]}"(dma), "{v91}"(min(lane * 16, 240u)), "{v92}"(lane * 16), [fb] "s"(fb), [fl] "s"(fl), [w2k] "s"(w2k), [gw] "s"(gw)
               : "v0", "v1", "v2", "v3", "v4", "s46", "s47", "scc", "memory");
  __builtin_amdgcn_sched_barrier(0);

  f32x32 acc, x0, x1, x2, x3, x4;
  u32x16 pd, wb;
  u32x8 hb;
  unsigned l16 = lane * 16, bq = b, wq = w;
  const unsigned q4 = (lane & 15) >> 2, tro = ((lane >> 4) & 1) * 32 + (lane & 1) * 16 + (lane & 2) * 4;
  const unsigned xa = 2 * SL + (32 * rw + t) * 128 + ((hh ^ ((t >> 1) & 7)) << 4);
  const unsigned wa = g < 15 ? 2 * SL + 16384 + (8 * hh + q4) * 256 + (((2 * cw) ^ q4) << 6) + tro : 32768 + hh * 16;
  u32x4 xav = {xa, xa ^ 32, xa ^ 64, xa ^ 96};
  u32x2 wav = {wa, wa ^ 64};
  u32x4 rp;
  rp[3] = HBO + (b == LEAD ? lane & 7 : xcd) * 128;
  rp[0] = (8 * hh + q4) * 64 + tro;
#define A1_REGS "+{s[40:41]}"(xb), "+{s[42:43]}"(qb), "+{v[68:71]}"(xav), "+{v[72:73]}"(wav), "+{v75}"(l16), "+{v[64:67]}"(dma), "+{v[84:87]}"(rp)
#define A1_LIVE "+{v[0:31]}"(acc), "+{v[32:47]}"(wb), "+{v[88:95]}"(hb), "+{v[96:127]}"(x0), "+{v[128:159]}"(x1), "+{v[160:191]}"(x2), \
                "+{v[192:223]}"(x3), "+{v[224:255]}"(x4), A1_REGS
  asm volatile(A1a
               : "={v[0:31]}"(acc), "={v[32:47]}"(wb), "={v[88:95]}"(hb), "={v[96:127]}"(x0), "={v[128:159]}"(x1), "={v[160:191]}"(x2),
                 "={v[192:223]}"(x3), "={v[224:255]}"(x4), A1_REGS
               : [fb] "s"(fb), [fl] "s"(fl), [w2k] "s"(w2k)
               : "scc", "memory" ST_CL);
  asm volatile("" : "+s"(bq), "+s"(wq));
  u32x4 eo;
  unsigned fk;
  {
    IDS(bq, wq);
    const unsigned ln = l16 >> 4, t = ln & 31, hh = ln >> 5;
    unsigned km = 0;
    if (g < 10) {
      const bool isk = g >= 5;
      const unsigned s0 = (isk ? g - 5 : g) + 3 * cw, o0 = 16 * (isk ? g - 5 : g) + 8 * cw + hh;
      const unsigned cb = isk ? 64 * KVT - 1536 : 0, ca = (isk ? 128 * KVT : 64 * 2560) - 2560 - cb;
      const unsigned base = (isk ? WS_KV + T1 * KVT : WS_Q + T1 * 2560) + (isk ? swap23(t) : t) * 16 + o0 * 512;
#pragma unroll
      for (int i = 0; i < 4; i++)
        eo[i] = __umul24((o0 * 205 + 410 * i) >> 10, ca) + __umul24((o0 * 205 + 410 * (i + 1)) >> 10, cb) + base + 1024 * i;
      km = isk ? ((0x108400u >> s0) & 0xff00) | 1 << 7 : 1 << 16;
    } else if (g < 15) {
#pragma unroll
      for (int i = 0; i < 2; i++) {
        const unsigned col = 128 * (g - 10) + 64 * cw + 32 * i + swap23(t), head = __umul24(col, 3277) >> 16, d = col - 20 * head;
        const unsigned dm = d - 4 * (head & 1);
        const unsigned row = dm < 16u ? swap23(dm) : (d & 3) + 16;
        eo[2 * i] = WS_KV + __umul24(head * 64 + T1, KVT) + 1280 + hh * 320 + row * 16;
        eo[2 * i + 1] = eo[2 * i] + 704;
      }
    } else {
      eo = (u32x4)(WS_KV + __umul24((4 * wq + (ln >> 4)) * 64 + 4 * r + ((ln >> 2) & 3), KVT) + 1920 + (ln & 2) * 352 + (ln & 1) * 16);
      eo[1] = WS_G + (T1 * 32 + t) * 4;
      km = 0xfc00;
    }
    fk = __builtin_amdgcn_readfirstlane(fl | km | (rw ? 0 : 1 << 17));
  }
  asm volatile(A1b : A1_LIVE, "+{v[76:79]}"(eo), [fl] "+s"(fk) : [fb] "s"(fb), [w2k] "s"(w2k) : "scc", "memory" ST_CL);
  asm volatile("" : "+s"(bq), "+s"(wq));
  u32x4 rb;
  {
    const unsigned f0 = bq * 8 + wq, f1 = f0 < 672 ? f0 + 2048 : f0;
    const unsigned ks0 = f0 / 20, cu0 = f0 - 20 * ks0, ks1 = f1 / 20, cu1 = f1 - 20 * ks1;
    const size_t p0 = (size_t)(ks0 < 96 ? w2 : wo - 96 * 20480) + (ks0 * 20480 + cu0 * 64);
    const size_t p1 = (size_t)(ks1 < 96 ? w2 : wo - 96 * 20480) + (ks1 * 20480 + cu1 * 64);
    rb = (u32x4){(unsigned)p0, (unsigned)(p0 >> 32), (unsigned)p1, (unsigned)(p1 >> 32)};
    rp[1] = WS_W + (cu0 * 136 + ks0) * 1024 + l16;
    rp[2] = WS_W + (cu1 * 136 + ks1) * 1024 + l16;
  }
  asm volatile(A1c : A1_LIVE, "+{v[76:79]}"(eo), "+{s[60:63]}"(rb) : [fb] "s"(fb), [fl] "s"(fk), [w2k] "s"(w2k) : "scc", "memory" ST_CL);
  asm volatile("" : "+s"(bq), "+s"(wq));
  mark_empty(ws, WS_O + (bq * 8 + wq) * 1280, l16);
  u32x8 sc;
  u32x4 sd;
  const char* kvb;
  {
    IDS(bq, wq);
    const size_t p[4] = {(size_t)ws + 64 * KVT, (size_t)ws + (WS_Q + (hp * 64 + Tq) * 2560), (size_t)b2 + c0 * 64, (size_t)b2 + c2 * 64};
#pragma unroll
    for (int i = 0; i < 4; i++) {
      sc[2 * i] = (unsigned)p[i];
      sc[2 * i + 1] = (unsigned)(p[i] >> 32);
    }
    sd = (u32x4){bq * 4, rep, odd * 1536, 1024 + odd * 8};
    kvb = ws + (WS_KV + head * 64 * KVT + wq * 3072);
  }
  asm volatile(A1d : A1_LIVE, "+{v[76:79]}"(eo), "+{s[60:63]}"(rb), "+{s[64:71]}"(sc), "+{s[72:75]}"(sd), "+s"(kvb)
               : [fb] "s"(fb), [fl] "s"(fk), [w2k] "s"(w2k) : "scc", "memory" ST_CL);
  asm volatile("" : "+s"(bq), "+s"(wq));
  {
  const unsigned b = bq, w = wq;
  IDS(b, w);
  const unsigned w3k = w * 3072;
  const char* w1b = w1 + g * 192;
  u32x4 mv;
  u32x8 se;
  unsigned oa, ot;
  unsigned w1a = rp[0] * 3 - (rp[0] & 63) * 2 + cw * 128;
  rp[0] += w2k;
  {
#pragma unroll
    for (int n = 0; n < 3; n++) {
      const unsigned i = 64 * (3 * wq + n) + (l16 >> 4);
      mv[n] = __umul24(__umul24(i, 43691) >> 19, 2880) + i * 16 - 1024 * n;
    }
    mv[3] = swap23((l16 >> 4) & 31) * 2;
    se = (u32x8){((idx & 7) * 80 + w * 10) * 3072, WS_H + (Tf * 96 + 24 * rw) * 1024, WS_W + (c0 * 136 + 24 * rw) * 1024,
                 WS_W + (c2 * 136 + 24 * rw) * 1024, WS_H + (T1 * 96 + 6 * g + 4 * cw) * 1024, WS_G + (Tq + cw) * 128, WS_G + Tf * 128,
                 (96 - 14 * rw) * 1024};
    oa = WS_O + ((Tq + cw) * 80 + 5 * hp) * 512 + 1024 + odd * 8;
    asm volatile("" : "+s"(oa));
    ot = oa;
    oa += odd * 1528 - 1024;
    asm volatile("" : "+s"(oa), "+s"(ot));
    asm volatile(A1e
                 : A1_LIVE, "={v[48:63]}"(pd), "+{v[76:79]}"(eo), "+{s[44:45]}"(w1b), "+{v74}"(w1a)
                 : "{v[80:83]}"(mv), [fb] "s"(fb), [fl] "s"(fk), [w2k] "s"(w2k), [w3k] "s"(w3k), [b1] "s"(b1 + (g * 192 + cw * 64)),
                   [ws] "s"(ws), "{s[76:83]}"(se)
                 : "s46", "s47", "scc", "memory" ST_CL);
  }

  const char* sb;
  unsigned wm;
  {
    unsigned cnt;
    asm volatile(A2
                 : "={v[0:31]}"(acc), "+{v74}"(w1a), "+{v[88:95]}"(hb), [cnt] "=&s"(cnt), "+{v[32:47]}"(wb), "+{v[48:63]}"(pd),
                   "+{v[96:127]}"(x0), "+{v[128:159]}"(x1), "+{v[160:191]}"(x2), "+{v[192:223]}"(x3), "+{s[42:43]}"(kvb),
                   "+{s[44:45]}"(w1b), "=&{s[40:41]}"(sb), "=&{s50}"(wm)
                 : "{v[224:255]}"(x4), "{v75}"(l16), "{v[76:79]}"(eo), "{v[84:87]}"(rp), "{v[80:83]}"(mv), "{v[64:67]}"(dma),
                   "{s[60:63]}"(rb), "{s[64:71]}"(sc), "{s[72:75]}"(sd), "{s[76:83]}"(se), [fb] "s"(fb), [fl] "s"(fk), [w2k] "s"(w2k),
                   [w3k] "s"(w3k), [ws] "s"(ws)
                 : "v68", "v69", "v70", "v71", "v72", "v73", "vcc", "scc", "memory" ST_CL);
  }

  asm volatile(A3
               : "+{v[0:31]}"(acc), "+{v[96:127]}"(x0), "+{v[128:159]}"(x1), "+{v[160:191]}"(x2), "+{v[192:223]}"(x3), "={v[224:255]}"(x4),
                 "+{s[40:41]}"(sb), "+{s[42:43]}"(kvb), "+{s[44:45]}"(w1b), "+{s50}"(wm)
               : [fb] "s"(fb), [fl] "s"(fl), [w3k] "s"(w3k), [slot] "s"(SLOT)
               : "v32", "v33", "v34", "v35", "v36", "v37", "v38", "v39", "v40", "v41", "v42", "v43", "v44", "v45", "v46", "v47", "v48", "v49", "v50", "v51", "v52", "v53", "v54", "v55", "v56", "v57", "v58", "v59", "v60", "v61", "v62", "v63", "v64", "v65", "v66", "v67", "v68", "v69", "v70", "v71", "v72", "v73", "v74", "v75", "v76", "v77", "v78", "v79", "v80", "v81", "v82", "v83", "v84", "v85", "v86", "v87", "v88", "v89", "v90", "v91", "v92", "v93", "v94", "v95", "s46", "s47", "s48", "s49", "s52", "s53", "vcc", "scc", "memory" ST_CL);
  f32x16 o = __builtin_shufflevector(acc, acc, 0, 1, 2, 3, 4, 5, 6, 7, 8, 9, 10, 11, 12, 13, 14, 15);
  {
    f32x2 z = (f32x2){o[0], o[1]} * 0.f;
#pragma unroll
    for (int i = 2; i < 12; i += 2) z = __builtin_elementwise_fma((f32x2){o[i], o[i + 1]}, (f32x2)(0.f), z);
    o[12] = upper_half(o[8]);
    const float lz = o[12] + (z[0] + z[1]);
    if (__builtin_expect(__builtin_amdgcn_ballot_w64(!(lz >= 0x1p-90f && lz <= 0x1p120f)) != 0, 0)) {
      const unsigned ln = lane_id(), t = ln & 31, hh = ln >> 5;
      const unsigned qo = WS_Q + (hp * 64 + Tq + cw) * 2560 + t * 16;
      o = attn_exact(ws, qo + (odd * 3 + hh) * 512, qo + 1024 + odd * 8, WS_KV + head * 64 * KVT, t, hh);
      o[12] = upper_half(o[8]);
    }
    asm volatile("" : "+{v[0:15]}"(o));
    const unsigned ln = lane_id(), t = ln & 31, hh = ln >> 5;
    const float l = o[12];
    const float sc = x3[20] * __builtin_amdgcn_rcpf(l);
    const unsigned lb = ln * 16;
    st16(ws, oa + lb, (u32x4){oword(o[0] * sc, o[1] * sc), oword(o[2] * sc, o[3] * sc), oword(o[4] * sc, o[5] * sc), oword(o[6] * sc, o[7] * sc)});
    if (hh == 0) st8(ws, ot + lb, (u32x2){oword(o[8] * sc, o[9] * sc), oword(o[10] * sc, o[11] * sc)});

    f32x32 wf0, wf1;
    f32x16 wf2;
    asm volatile(A4W : "=&{v[0:31]}"(wf0), "=&{v[32:63]}"(wf1), "=&{v[64:79]}"(wf2) : [wa] "v"(SLOT + rw * 10240 + lb) : "memory" ST_CL);
    u32x8 ae = {WS_O + Tf * 40960 + rw * 10240 + lb, lb, lb, rw * 2048 + cw * 49152 + lb,
                (rw + 6 * cw) * 8192 + lb, (Tf * 32 + t) * 1280 + (c0 * 32 + 16 * rw + 8 * hh) * 2, lb,
                (ra * 32 + t) * 1280 + (c2 * 32 + 16 * (rw & 1) + 8 * hh) * 2};
    sb = ws;
    asm volatile(A4
                 : "+{v[128:159]}"(x1), "+{v[160:191]}"(x2), "+{v[192:223]}"(x3), "+{v[120:127]}"(ae), "+{s[40:41]}"(sb),
                   "+{v[0:31]}"(wf0), "+{v[32:63]}"(wf1), "+{v[64:79]}"(wf2)
                 : "{v[224:255]}"(x4), [fb] "s"(fb), [fl] "s"(fl), [w3k] "s"(w3k), [out] "s"(out)
                 : "v80", "v81", "v82", "v83", "v84", "v85", "v86", "v87", "v88", "v89", "v90", "v91", "v92", "v93", "v94", "v95", "v96", "v97", "v98", "v99", "v100", "v101", "v102", "v103", "v104", "v105", "v106", "v107", "v108", "v109", "v110", "v111", "v112", "v113", "v114", "v115", "v116", "v117", "v118", "v119", "s48", "s52", "s53", "vcc", "scc", "memory" ST_CL);
  }
  }
}

extern "C" void kernel_launch(void* const* d_in, const int* in_sizes, int n_in, void* d_out, int out_size, void* d_ws, size_t ws_size,
                              hipStream_t stream) {
  (void)hipFuncSetAttribute((const void*)k_main, hipFuncAttributeMaxDynamicSharedMemorySize, LDS_BYTES);
  k_main<<<256, 512, LDS_BYTES, stream>>>((const char*)d_in[0], (const char*)d_in[1], (const char*)d_in[2], (const char*)d_in[4],
                                          (const char*)d_in[5], (const char*)d_in[6], (const char*)d_in[7], (const char*)d_in[8],
                                          (char*)d_ws, (char*)d_out);
}
```
